```python
import torch

def _softmax(x, axis=-1):
    return torch.softmax(x, dim=axis)

def _gelu(x):
    return torch.nn.functional.gelu(x, approximate='tanh')

def _silu(x):
    return torch.nn.functional.silu(x)

def _sigmoid(x):
    return torch.sigmoid(x)

def _layernorm(x, axis=-1, eps=1e-05):
    m = x.mean(dim=axis, keepdim=True)
    v = x.var(dim=axis, keepdim=True, unbiased=False)
    return (x - m) / torch.sqrt(v + eps)

def _scatter_add(tgt, idx, src, axis=0):
    return tgt.clone().index_add_(axis, idx, src)

def _segment_sum(src, seg, n):
    out = torch.zeros((n,) + src.shape[1:], dtype=src.dtype)
    return out.index_add_(0, seg, src)

def _pad(x, pw):
    flat = tuple((v for pair in reversed(pw) for v in pair))
    return torch.nn.functional.pad(x, flat)
kernel_name = 'gated_mix__spectral_fnet__mixer_gmlp__prompt'

def setup_inputs():

    _w = lambda a: (lambda t: t.to(torch.bfloat16) if t.is_floating_point() else t)(torch.as_tensor(a))

    def _A_setup():
        B, S, D = (1, 2048, 448)
        ns = torch.arange(S)[:, None] * torch.arange(S)[None, :]
        nd = torch.arange(D)[:, None] * torch.arange(D)[None, :]
        Ws_re = torch.cos(-2.0 * torch.pi * ns / S).to(float)
        Ws_im = torch.sin(-2.0 * torch.pi * ns / S).to(float)
        Wd_re = torch.cos(-2.0 * torch.pi * nd / D).to(float)
        Wd_im = torch.sin(-2.0 * torch.pi * nd / D).to(float)
        return {'x': _w((lambda *s: torch.randn(*s, dtype=torch.bfloat16) * 0.05)(B, S, D)), 'dft_seq_re': _w(Ws_re), 'dft_seq_im': _w(Ws_im), 'dft_hid_re': _w(Wd_re), 'dft_hid_im': _w(Wd_im)}
    _A_in = _A_setup()

    def _B_setup():
        B, S, D, F = (1, 2048, 448, 1792)
        Fh = F // 2
        return {'x': _w((lambda *s: torch.randn(*s, dtype=torch.bfloat16) * 0.05)(B, S, D)), 'w_in': _w((lambda *s: torch.randn(*s, dtype=torch.bfloat16) * 0.8)(D, 2 * Fh)), 'w_sp': _w(0.01 * (lambda *s: torch.randn(*s, dtype=torch.bfloat16) * 0.8)(S, S)), 'b_sp': _w(1.0 + 0.01 * (lambda *s: torch.randn(*s, dtype=torch.bfloat16) * 0.8)(S)), 'w_out': _w((lambda *s: torch.randn(*s, dtype=torch.bfloat16) * 0.8)(Fh, D))}
    _B_in = _B_setup()
    return {'A_x': _A_in['x'], 'A_dft_seq_re': _A_in['dft_seq_re'], 'A_dft_seq_im': _A_in['dft_seq_im'], 'A_dft_hid_re': _A_in['dft_hid_re'], 'A_dft_hid_im': _A_in['dft_hid_im'], 'B_x': _B_in['x'], 'B_w_in': _B_in['w_in'], 'B_w_sp': _B_in['w_sp'], 'B_b_sp': _B_in['b_sp'], 'B_w_out': _B_in['w_out'], 'gate_w': _w((lambda *s: torch.randn(*s, dtype=torch.bfloat16) * 0.05)(448) * 16.0)}

def reference(A_x, A_dft_seq_re, A_dft_seq_im, A_dft_hid_re, A_dft_hid_im, B_x, B_w_in, B_w_sp, B_b_sp, B_w_out, gate_w):

    def _A_ref(x, dft_seq_re, dft_seq_im, dft_hid_re, dft_hid_im):
        a = torch.einsum('bsd,ed->bse', x, dft_hid_re)
        b = torch.einsum('bsd,ed->bse', x, dft_hid_im)
        y = torch.einsum('ts,bse->bte', dft_seq_re, a) - torch.einsum('ts,bse->bte', dft_seq_im, b)
        return y

    def _B_ref(x, w_in, w_sp, b_sp, w_out):
        B, S, D = x.shape
        F = w_in.shape[-1]
        Fh = F // 2
        z = _gelu(torch.einsum('bsd,df->bsf', x, w_in))
        z1 = z[..., :Fh].clone()
        z2 = _layernorm(z[..., Fh:], axis=-1)
        g = torch.einsum('ts,bsf->btf', w_sp, z2) + b_sp[None, :, None]
        return torch.einsum('bsf,fd->bsd', z1 * g, w_out)
    x = A_x
    g = _sigmoid((x * gate_w).sum(-1, keepdims=True))
    a = _A_ref(x, A_dft_seq_re, A_dft_seq_im, A_dft_hid_re, A_dft_hid_im)
    b = _B_ref(x, B_w_in, B_w_sp, B_b_sp, B_w_out)
    return g * a + (1 - g) * b
```

```cpp
#include <hip/hip_runtime.h>
#include <hip/hip_bf16.h>
typedef unsigned short u16;
__device__ __forceinline__ float bf2f(u16 v){ return __uint_as_float(((unsigned)v)<<16); }
__device__ __forceinline__ u16 f2bf(float f){ __hip_bfloat16 b = __float2bfloat16(f); return *(u16*)&b; }

__global__ void gemm_naive(const u16* A, int am, int ak, const u16* B, int bk, int bn, float* C, int ldc, int M, int N, int K){
  __shared__ float sa[16][17], sb[16][17];
  int tx = threadIdx.x & 15, ty = threadIdx.x >> 4;
  int m = blockIdx.y*16 + ty, n = blockIdx.x*16 + tx;
  float acc = 0.f;
  for (int k0 = 0; k0 < K; k0 += 16){
    sa[ty][tx] = bf2f(A[(size_t)m*am + (size_t)(k0+tx)*ak]);
    sb[ty][tx] = bf2f(B[(size_t)(k0+ty)*bk + (size_t)n*bn]);
    __syncthreads();
    for (int k = 0; k < 16; ++k) acc += sa[ty][k]*sb[k][tx];
    __syncthreads();
  }
  C[(size_t)m*ldc + n] = acc;
}
__device__ float gelu_t(float x){ float u = 0.7978845608f*(x + 0.044715f*x*x*x); return 0.5f*x*(1.f + tanhf(u)); }
__global__ void row_ops(const float* pre, const float* ab, const u16* x, const u16* gw, u16* z1, u16* z2, u16* abh, float* gate){
  int s = blockIdx.x; int t = threadIdx.x;
  __shared__ float red[256]; __shared__ float zz[896];
  for (int f = t; f < 896; f += 256){ z1[s*896+f] = f2bf(gelu_t(pre[s*1792+f])); zz[f] = gelu_t(pre[s*1792+896+f]); abh[s*896+f] = f2bf(ab[s*896+f]); }
  __syncthreads();
  float p = 0; for (int f = t; f < 896; f += 256) p += zz[f];
  red[t] = p; __syncthreads(); for (int o = 128; o > 0; o >>= 1){ if (t < o) red[t] += red[t+o]; __syncthreads(); }
  float mean = red[0]/896.f; __syncthreads();
  p = 0; for (int f = t; f < 896; f += 256){ float d = zz[f]-mean; p += d*d; }
  red[t] = p; __syncthreads(); for (int o = 128; o > 0; o >>= 1){ if (t < o) red[t] += red[t+o]; __syncthreads(); }
  float rstd = rsqrtf(red[0]/896.f + 1e-5f); __syncthreads();
  for (int f = t; f < 896; f += 256) z2[s*896+f] = f2bf((zz[f]-mean)*rstd);
  p = 0; for (int d = t; d < 448; d += 256) p += bf2f(x[s*448+d])*bf2f(gw[d]);
  red[t] = p; __syncthreads(); for (int o = 128; o > 0; o >>= 1){ if (t < o) red[t] += red[t+o]; __syncthreads(); }
  if (t == 0) gate[s] = 1.f/(1.f + __expf(-red[0]));
}
__global__ void h_op(const float* g, const u16* z1, const u16* bsp, u16* h){
  int i = blockIdx.x*256 + threadIdx.x; int t = i/896;
  h[i] = f2bf(bf2f(z1[i])*(g[i] + bf2f(bsp[t])));
}
__global__ void fin(const float* yre, const float* yim, const float* ob, const float* gate, u16* out){
  int i = blockIdx.x*256 + threadIdx.x; int t = i/448; float g = gate[t];
  out[i] = f2bf(g*(yre[i]-yim[i]) + (1.f-g)*ob[i]);
}
extern "C" void kernel_launch(void* const* d_in, const int* in_sizes, int n_in, void* d_out, int out_size, void* d_ws, size_t ws_size, hipStream_t stream){
  const u16* x = (const u16*)d_in[0]; const u16* wsr = (const u16*)d_in[1]; const u16* wsi = (const u16*)d_in[2];
  const u16* wdr = (const u16*)d_in[3]; const u16* wdi = (const u16*)d_in[4];
  const u16* win = (const u16*)d_in[6]; const u16* wsp = (const u16*)d_in[7]; const u16* bsp = (const u16*)d_in[8];
  const u16* wout = (const u16*)d_in[9]; const u16* gw = (const u16*)d_in[10];
  char* ws = (char*)d_ws;
  float* pre = (float*)ws; ws += 2048*1792*4;
  float* ab = (float*)ws; ws += 2048*896*4;
  float* g = (float*)ws; ws += 2048*896*4;
  float* yre = (float*)ws; ws += 2048*448*4;
  float* yim = (float*)ws; ws += 2048*448*4;
  float* ob = (float*)ws; ws += 2048*448*4;
  float* gate = (float*)ws; ws += 2048*4;
  u16* z1 = (u16*)ws; ws += 2048*896*2;
  u16* z2 = (u16*)ws; ws += 2048*896*2;
  u16* abh = (u16*)ws; ws += 2048*896*2;
  u16* h = (u16*)ws; ws += 2048*896*2;
  gemm_naive<<<dim3(1792/16,128),256,0,stream>>>(x,448,1, win,1792,1, pre,1792, 2048,1792,448);
  gemm_naive<<<dim3(448/16,128),256,0,stream>>>(x,448,1, wdr,1,448, ab,896, 2048,448,448);
  gemm_naive<<<dim3(448/16,128),256,0,stream>>>(x,448,1, wdi,1,448, ab+448,896, 2048,448,448);
  row_ops<<<2048,256,0,stream>>>(pre, ab, x, gw, z1, z2, abh, gate);
  gemm_naive<<<dim3(448/16,128),256,0,stream>>>(wsr,2048,1, abh,896,1, yre,448, 2048,448,2048);
  gemm_naive<<<dim3(448/16,128),256,0,stream>>>(wsi,2048,1, abh+448,896,1, yim,448, 2048,448,2048);
  gemm_naive<<<dim3(896/16,128),256,0,stream>>>(wsp,2048,1, z2,896,1, g,896, 2048,896,2048);
  h_op<<<2048*896/256,256,0,stream>>>(g, z1, bsp, h);
  gemm_naive<<<dim3(448/16,128),256,0,stream>>>(h,896,1, wout,448,1, ob,448, 2048,448,896);
  fin<<<2048*448/256,256,0,stream>>>(yre, yim, ob, gate, (u16*)d_out);
}
```

```cpp
#include <hip/hip_runtime.h>
#include <hip/hip_bf16.h>

#define LAS __attribute__((address_space(3)))
#define GAS __attribute__((address_space(1)))
typedef unsigned short u16;
typedef unsigned int u32;
typedef unsigned long long u64;
typedef short bf16x8 __attribute__((ext_vector_type(8)));
typedef short bf16x4 __attribute__((ext_vector_type(4)));
typedef float f32x4 __attribute__((ext_vector_type(4)));
typedef float f32x2 __attribute__((ext_vector_type(2)));
typedef unsigned u32x4 __attribute__((ext_vector_type(4)));
typedef unsigned u32x2 __attribute__((ext_vector_type(2)));
typedef __bf16 bf2_t __attribute__((ext_vector_type(2)));

#define DI __device__ __forceinline__
#define MFMA(a, b, c) __builtin_amdgcn_mfma_f32_16x16x32_bf16((a), (b), (c), 0, 0, 0)
#define WAIT_V(n) asm volatile("s_waitcnt vmcnt(%0)" ::"i"(n) : "memory")
#define WAIT_L0() asm volatile("s_waitcnt lgkmcnt(0)" ::: "memory")
#define BAR() do { asm volatile("" ::: "memory"); __builtin_amdgcn_s_barrier(); asm volatile("" ::: "memory"); } while (0)
#define SCHED() __builtin_amdgcn_sched_barrier(0)
#define GLDS(g, l) __builtin_amdgcn_global_load_lds((const u32*)(g), (LAS u32*)(l), 16, 0, 0)

constexpr size_t BMAT_C = 458752;
constexpr size_t OFF_BMAT = 0;
constexpr size_t OFF_Z1 = OFF_BMAT + 16 * BMAT_C;
constexpr size_t OFF_OMAT = OFF_Z1 + 128 * 28 * 1024;
constexpr size_t OFF_GATE = OFF_OMAT + 128 * 56 * 1024;
constexpr size_t OFF_EPOCH = OFF_GATE + 2048 * 4;
constexpr size_t OFF_XCH = OFF_EPOCH + 16 * 128;
constexpr size_t WS_END = OFF_XCH + 16 * 8 * 128 * 16;

DI float bf2f(u16 v) { return __uint_as_float(((u32)v) << 16); }
DI u32 pk2(float lo, float hi) { bf2_t v = {(__bf16)lo, (__bf16)hi}; return __builtin_bit_cast(u32, v); }
DI float blo(u32 v) { return __uint_as_float(v << 16); }
DI float bhi(u32 v) { return __uint_as_float(v & 0xffff0000u); }
DI float gelu_t(float x) {
  const float a = x * __builtin_fmaf(x * x, -0.1029432397f, -2.302208198f);
  return x * __builtin_amdgcn_rcpf(1.0f + __builtin_amdgcn_exp2f(a));
}
DI int rowperm(int rho) { return 32 * (rho >> 5) + 8 * ((rho & 15) >> 2) + 4 * ((rho >> 4) & 1) + (rho & 3); }
DI int swap23(int v) { return (v & ~12) | ((v & 4) << 1) | ((v & 8) >> 1); }
DI void decode_rc(int b, int& r, int& c) {
  const int xcd = b & 7, idx = b >> 3;
  if (xcd < 4) { r = 4 * xcd + (idx >> 3); c = idx & 7; }
  else if (xcd < 6) { r = 8 * (xcd - 4) + (idx >> 2); c = 8 + (idx & 3); }
  else { r = 8 * (xcd - 6) + (idx >> 2); c = 12 + (idx & 3); }
}
DI float row16_sum(float v) {
  v += __builtin_bit_cast(float, __builtin_amdgcn_update_dpp(0, __builtin_bit_cast(int, v), 0x128, 0xf, 0xf, false));
  v += __builtin_bit_cast(float, __builtin_amdgcn_update_dpp(0, __builtin_bit_cast(int, v), 0x124, 0xf, 0xf, false));
  v += __builtin_bit_cast(float, __builtin_amdgcn_update_dpp(0, __builtin_bit_cast(int, v), 0x122, 0xf, 0xf, false));
  v += __builtin_bit_cast(float, __builtin_amdgcn_update_dpp(0, __builtin_bit_cast(int, v), 0x121, 0xf, 0xf, false));
  return v;
}
DI float quad_sum(float v) {
  v += __builtin_bit_cast(float, __builtin_amdgcn_update_dpp(0, __builtin_bit_cast(int, v), 0xB1, 0xf, 0xf, false));
  v += __builtin_bit_cast(float, __builtin_amdgcn_update_dpp(0, __builtin_bit_cast(int, v), 0x4E, 0xf, 0xf, false));
  return v;
}
DI bf16x8 ldsr(const char* p) { return *(const LAS bf16x8*)(const LAS char*)p; }
DI bf16x8 lds_tr2(const char* p0, const char* p1) {
  bf16x4 a = __builtin_amdgcn_ds_read_tr16_b64_v4i16((LAS bf16x4*)(LAS char*)p0);
  bf16x4 b = __builtin_amdgcn_ds_read_tr16_b64_v4i16((LAS bf16x4*)(LAS char*)p1);
  return __builtin_shufflevector(a, b, 0, 1, 2, 3, 4, 5, 6, 7);
}

DI void store_pairs(char* base, int c7, const u32x2 (&v)[7]) {
  char* p = base + (size_t)(c7 >> 1) * 1024;
  if ((c7 & 1) == 0) {
#pragma unroll
    for (int k = 0; k < 3; ++k) *(u32x4*)(p + k * 1024) = u32x4{v[2 * k].x, v[2 * k].y, v[2 * k + 1].x, v[2 * k + 1].y};
    *(u32x2*)(p + 3 * 1024) = v[6];
  } else {
    *(u32x2*)(p + 8) = v[0];
#pragma unroll
    for (int k = 0; k < 3; ++k) *(u32x4*)(p + (k + 1) * 1024) = u32x4{v[2 * k + 1].x, v[2 * k + 1].y, v[2 * k + 2].x, v[2 * k + 2].y};
  }
}
DI void load_pairs(const char* base, int c7, u32x2 (&v)[7]) {
  const char* p = base + (size_t)(c7 >> 1) * 1024;
  if ((c7 & 1) == 0) {
#pragma unroll
    for (int k = 0; k < 3; ++k) { u32x4 t = *(const u32x4*)(p + k * 1024); v[2 * k] = u32x2{t.x, t.y}; v[2 * k + 1] = u32x2{t.z, t.w}; }
    v[6] = *(const u32x2*)(p + 3 * 1024);
  } else {
    v[0] = *(const u32x2*)(p + 8);
#pragma unroll
    for (int k = 0; k < 3; ++k) { u32x4 t = *(const u32x4*)(p + (k + 1) * 1024); v[2 * k + 1] = u32x2{t.x, t.y}; v[2 * k + 2] = u32x2{t.z, t.w}; }
  }
}

constexpr int K1_LDS = 153600 + 1024 + 4096;
__global__ __launch_bounds__(512, 2) void k1(const char* __restrict__ x, const char* __restrict__ win, const char* __restrict__ wdr,
                                             const char* __restrict__ wdi, const char* __restrict__ gw, char* ws) {
  __shared__ __attribute__((aligned(1024))) char smem[K1_LDS];
  int r, c; decode_rc(blockIdx.x, r, c);
  const int tid = threadIdx.x, wid = __builtin_amdgcn_readfirstlane(tid >> 6), lane = tid & 63;
  const int q = lane >> 4, i16 = lane & 15;
  u32 xoff[2];
#pragma unroll
  for (int k = 0; k < 2; ++k) { const int rho = 8 * (2 * wid + k) + (lane >> 3); xoff[k] = (u32)((128 * r + rowperm(rho)) * 896 + (((lane & 7) ^ ((rho >> 1) & 7)) << 4)); }
  const int zc = (c < 8) ? (896 + 112 * c) : 112 * (c - 8);
  u32 woff[2];
#pragma unroll
  for (int k = 0; k < 2; ++k) { int P = 112 * wid + 64 * k + lane; if (k == 1 && lane >= 48) P = 112 * wid; const int rho = P / 14, ch = P - rho * 14; woff[k] = (u32)(swap23(rho) * 3584 + zc * 2 + ch * 16); }
  const int mw = wid & 3;
  const int a0 = (32 * mw + i16) * 128 + ((q ^ ((i16 >> 1) & 7)) << 4);
  const int a1 = a0 ^ 64;
  const int trb = (16 * (q >> 1) + 4 * (q & 1) + (i16 >> 2)) * 224 + (lane & 3) * 8;

  if (c < 8) {
    constexpr int SB = 30720, NS = 5;
    GAS u32* ep = (GAS u32*)(ws + OFF_EPOCH + r * 128);
    const u32 tag = __hip_atomic_load(ep, __ATOMIC_RELAXED, __HIP_MEMORY_SCOPE_AGENT) + 1u;
    auto issue = [&](int s) {
      char* sl = smem + (s % NS) * SB;
      GLDS(x + s * 128 + xoff[0], sl + (2 * wid) * 1024);
      GLDS(x + s * 128 + xoff[1], sl + (2 * wid + 1) * 1024);
      GLDS(win + (size_t)s * (64 * 3584) + woff[0], sl + 16384 + wid * 1792);
      if (lane < 48) GLDS(win + (size_t)s * (64 * 3584) + woff[1], sl + 16384 + wid * 1792 + 1024);
    };
#pragma unroll
    for (int s = 0; s < NS; ++s) issue(s);
    const int nh = wid >> 2;
    f32x4 acc[2][4];
#pragma unroll
    for (int m = 0; m < 2; ++m)
#pragma unroll
      for (int j = 0; j < 4; ++j) acc[m][j] = f32x4{0.f, 0.f, 0.f, 0.f};
#pragma unroll
    for (int s = 0; s < 7; ++s) {
      const int issued = (NS + (s > 1 ? s - 1 : 0)) > 7 ? 7 : (NS + (s > 1 ? s - 1 : 0));
      WAIT_V(4 * (issued - s - 1));
      WAIT_L0();
      BAR();
      if (s >= 1 && s - 1 + NS < 7) issue(s - 1 + NS);
      const char* sl = smem + (s % NS) * SB;
      const char* tw = sl + 16384 + trb + nh * 128;
#pragma unroll
      for (int kb = 0; kb < 2; ++kb) {
        const bf16x8 xf0 = ldsr(sl + (kb ? a1 : a0)), xf1 = ldsr(sl + (kb ? a1 : a0) + 2048);
#pragma unroll
        for (int j = 0; j < 4; ++j) {
          if (j < 3 || nh == 0) {
            const bf16x8 wf = lds_tr2(tw + (32 * kb) * 224 + 32 * j, tw + (32 * kb + 8) * 224 + 32 * j);
            acc[0][j] = MFMA(xf0, wf, acc[0][j]);
            acc[1][j] = MFMA(xf1, wf, acc[1][j]);
          }
        }
      }
    }
    const int nj = nh ? 3 : 4;
    const float cnt = nh ? 48.f : 64.f, icnt = nh ? (1.f / 48.f) : (1.f / 64.f);
    LAS f32x2* part = (LAS f32x2*)(LAS char*)(smem + 153600 + 1024);
    LAS f32x2* fin = part + 256;
#pragma unroll
    for (int m = 0; m < 2; ++m)
#pragma unroll
      for (int e = 0; e < 4; ++e) {
        float s1 = 0.f;
#pragma unroll
        for (int j = 0; j < 4; ++j) { float g = gelu_t(acc[m][j][e]); if (j >= nj) g = 0.f; acc[m][j][e] = g; s1 += g; }
        const float mean = row16_sum(s1) * icnt;
        float s2 = 0.f;
#pragma unroll
        for (int j = 0; j < 4; ++j) { const float d = acc[m][j][e] - mean; if (j < nj) s2 += d * d; }
        s2 = row16_sum(s2);
        if (i16 == 0) part[(32 * mw + 8 * q + 4 * m + e) * 2 + nh] = f32x2{mean, s2};
      }
    (void)cnt;
    WAIT_L0();
    BAR();
    char* xch = ws + OFF_XCH + (size_t)r * (8 * 128 * 16);
    if (tid < 128) {
      const f32x2 pa = part[tid * 2], pb = part[tid * 2 + 1];
      const float mean = (pa.x * 64.f + pb.x * 48.f) * (1.f / 112.f);
      const float dl = pa.x - pb.x;
      const float m2 = pa.y + pb.y + dl * dl * (64.f * 48.f / 112.f);
      __amdgpu_buffer_rsrc_t rs = __builtin_amdgcn_make_buffer_rsrc(xch, 0, 8 * 128 * 16, 0x00020000);
      __builtin_amdgcn_raw_buffer_store_b128(u32x4{__float_as_uint(mean), tag, __float_as_uint(m2), tag}, rs, c * 2048 + tid * 16, 0, 16);
    }
    {
      __amdgpu_buffer_rsrc_t rs = __builtin_amdgcn_make_buffer_rsrc(xch, 0, 8 * 128 * 16, 0x00020000);
      const int row = tid >> 2, g = tid & 3;
      u32x4 ga, gb;
      const u64 t0 = __builtin_amdgcn_s_memrealtime();
      for (;;) {
        ga = __builtin_amdgcn_raw_buffer_load_b128(rs, (2 * g) * 2048 + row * 16, 0, 16);
        gb = __builtin_amdgcn_raw_buffer_load_b128(rs, (2 * g + 1) * 2048 + row * 16, 0, 16);
        const bool ok = ga.y == tag && ga.w == tag && gb.y == tag && gb.w == tag;
        if (__all(ok)) break;
        if (__builtin_amdgcn_s_memrealtime() - t0 > 5000000ull) break;
        __builtin_amdgcn_s_sleep(1);
      }
      const float ma = __uint_as_float(ga.x), mb = __uint_as_float(gb.x);
      const float mean = quad_sum(ma + mb) * 0.125f;
      const float da = ma - mean, db = mb - mean;
      const float m2 = quad_sum(__uint_as_float(ga.z) + __uint_as_float(gb.z) + 112.f * (da * da + db * db));
      if (g == 0) fin[row] = f32x2{mean, __builtin_amdgcn_rsqf(m2 * (1.f / 896.f) + 1e-5f)};
    }
    WAIT_L0();
    BAR();
    if (c == 0 && tid == 0) __hip_atomic_store(ep, tag, __ATOMIC_RELAXED, __HIP_MEMORY_SCOPE_AGENT);
    f32x2 st[8];
#pragma unroll
    for (int e = 0; e < 8; ++e) st[e] = fin[32 * mw + 8 * q + e];
    char* bo = ws + OFF_BMAT + (size_t)c * BMAT_C + (size_t)((4 * r + mw) * 7 + 4 * nh) * 1024 + lane * 16;
#pragma unroll
    for (int j = 0; j < 4; ++j) {
      if (j < nj) {
        float v[8];
#pragma unroll
        for (int e = 0; e < 8; ++e) v[e] = (acc[e >> 2][j][e & 3] - st[e].x) * st[e].y;
        *(u32x4*)(bo + j * 1024) = u32x4{pk2(v[0], v[1]), pk2(v[2], v[3]), pk2(v[4], v[5]), pk2(v[6], v[7])};
      }
    }
  } else {
    constexpr int SB = 45056, NS = 3;
    const char* wd = (c < 12) ? wdr : wdi;
    u32 doff[2];
#pragma unroll
    for (int k = 0; k < 2; ++k) { int P = 112 * wid + 64 * k + lane; if (k == 1 && lane >= 48) P = 112 * wid; const int e = P >> 3; doff[k] = (u32)((112 * (c & 3) + e) * 896 + (((P & 7) ^ ((e >> 1) & 7)) << 4)); }
    if (wid == 0 && lane < 56) GLDS(gw + lane * 16, smem + 153600);
    auto issue = [&](int s) {
      char* sl = smem + (s % NS) * SB;
      GLDS(x + s * 128 + xoff[0], sl + (2 * wid) * 1024);
      GLDS(x + s * 128 + xoff[1], sl + (2 * wid + 1) * 1024);
      GLDS(wd + s * 128 + doff[0], sl + 16384 + wid * 1792);
      GLDS(win + (size_t)s * (64 * 3584) + woff[0], sl + 30720 + wid * 1792);
      if (lane < 48) {
        GLDS(wd + s * 128 + doff[1], sl + 16384 + wid * 1792 + 1024);
        GLDS(win + (size_t)s * (64 * 3584) + woff[1], sl + 30720 + wid * 1792 + 1024);
      }
    };
#pragma unroll
    for (int s = 0; s < NS; ++s) issue(s);
    f32x4 acc[2][7], gacc[2];
#pragma unroll
    for (int m = 0; m < 2; ++m) {
      gacc[m] = f32x4{0.f, 0.f, 0.f, 0.f};
#pragma unroll
      for (int j = 0; j < 7; ++j) acc[m][j] = f32x4{0.f, 0.f, 0.f, 0.f};
    }
    const int d0 = i16 * 128 + ((q ^ ((i16 >> 1) & 7)) << 4);
#pragma unroll
    for (int s = 0; s < 7; ++s) {
      const int issued = (NS + (s > 1 ? s - 1 : 0)) > 7 ? 7 : (NS + (s > 1 ? s - 1 : 0));
      WAIT_V(6 * (issued - s - 1));
      WAIT_L0();
      BAR();
      if (s >= 1 && s - 1 + NS < 7) issue(s - 1 + NS);
      const char* sl = smem + (s % NS) * SB;
      if (wid < 4) {
#pragma unroll
        for (int kb = 0; kb < 2; ++kb) {
          const bf16x8 xf0 = ldsr(sl + (kb ? a1 : a0)), xf1 = ldsr(sl + (kb ? a1 : a0) + 2048);
          const bf16x8 gf = ldsr(smem + 153600 + s * 128 + kb * 64 + q * 16);
          gacc[0] = MFMA(xf0, gf, gacc[0]);
          gacc[1] = MFMA(xf1, gf, gacc[1]);
#pragma unroll
          for (int j = 0; j < 7; ++j) {
            const bf16x8 wf = ldsr(sl + 16384 + (kb ? (d0 ^ 64) : d0) + j * 2048);
            acc[0][j] = MFMA(xf0, wf, acc[0][j]);
            acc[1][j] = MFMA(xf1, wf, acc[1][j]);
          }
        }
      } else {
        const char* tw = sl + 30720 + trb;
#pragma unroll
        for (int kb = 0; kb < 2; ++kb) {
          const bf16x8 xf0 = ldsr(sl + (kb ? a1 : a0)), xf1 = ldsr(sl + (kb ? a1 : a0) + 2048);
#pragma unroll
          for (int j = 0; j < 7; ++j) {
            const bf16x8 wf = lds_tr2(tw + (32 * kb) * 224 + 32 * j, tw + (32 * kb + 8) * 224 + 32 * j);
            acc[0][j] = MFMA(wf, xf0, acc[0][j]);
            acc[1][j] = MFMA(wf, xf1, acc[1][j]);
          }
        }
      }
    }
    if (wid < 4) {
      char* bo = ws + OFF_BMAT + (size_t)c * BMAT_C + (size_t)((4 * r + mw) * 7) * 1024 + lane * 16;
#pragma unroll
      for (int j = 0; j < 7; ++j)
        *(u32x4*)(bo + j * 1024) = u32x4{pk2(acc[0][j][0], acc[0][j][1]), pk2(acc[0][j][2], acc[0][j][3]), pk2(acc[1][j][0], acc[1][j][1]), pk2(acc[1][j][2], acc[1][j][3])};
      if (c == 8 && i16 == 0) {
        float* gp = (float*)(ws + OFF_GATE) + 128 * r + 32 * mw + 8 * q;
        f32x4 g0, g1;
#pragma unroll
        for (int e = 0; e < 4; ++e) {
          g0[e] = __builtin_amdgcn_rcpf(1.0f + __builtin_amdgcn_exp2f(-1.442695041f * gacc[0][e]));
          g1[e] = __builtin_amdgcn_rcpf(1.0f + __builtin_amdgcn_exp2f(-1.442695041f * gacc[1][e]));
        }
        *(f32x4*)gp = g0; *(f32x4*)(gp + 4) = g1;
      }
    } else {
#pragma unroll
      for (int m = 0; m < 2; ++m) {
        u32x2 v[7];
#pragma unroll
        for (int j = 0; j < 7; ++j) v[j] = u32x2{pk2(gelu_t(acc[m][j][0]), gelu_t(acc[m][j][1])), pk2(gelu_t(acc[m][j][2]), gelu_t(acc[m][j][3]))};
        store_pairs(ws + OFF_Z1 + (size_t)(8 * r + 2 * mw + m) * (28 * 1024) + lane * 16, 7 * (c - 8), v);
      }
    }
  }
}

constexpr int K2_SB = 30720, K2_NS = 5;
__global__ __launch_bounds__(512, 2) void k2(const char* __restrict__ wsp, const char* __restrict__ wsr, const char* __restrict__ wsi,
                                             const u16* __restrict__ bsp, char* ws) {
  __shared__ __attribute__((aligned(1024))) char smem[K2_SB * K2_NS];
  int r, c; decode_rc(blockIdx.x, r, c);
  const int tid = threadIdx.x, wid = __builtin_amdgcn_readfirstlane(tid >> 6), lane = tid & 63;
  if (wid >= 4) {
    const int lw = wid - 4;
    const char* W = (c < 8) ? wsp : (c < 12) ? wsr : wsi;
    u32 aoff[4];
#pragma unroll
    for (int k = 0; k < 4; ++k) { const int rho = 8 * (4 * lw + k) + (lane >> 3); aoff[k] = (u32)((128 * r + rowperm(rho)) * 4096 + (((lane & 7) ^ ((rho >> 1) & 7)) << 4)); }
    const char* Bm = ws + OFF_BMAT + (size_t)c * BMAT_C + lw * 3584;
    const u32 boff = lane * 16;
    auto issue = [&](int s, int slot) {
      char* sl = smem + slot * K2_SB;
      const char* wa = W + s * 128;
      const char* wb = Bm + (size_t)s * 14336;
#pragma unroll
      for (int k = 0; k < 4; ++k) GLDS(wa + aoff[k], sl + (4 * lw + k) * 1024);
#pragma unroll
      for (int k = 0; k < 3; ++k) GLDS(wb + k * 1024 + boff, sl + 16384 + lw * 3584 + k * 1024);
      if (lane < 32) GLDS(wb + 3072 + boff, sl + 16384 + lw * 3584 + 3072);
    };
#pragma unroll
    for (int s = 0; s < K2_NS; ++s) issue(s, s);
    WAIT_V(32);
    BAR();
    int slot = 0;
    for (int t = 0; t < 27; ++t) {
      WAIT_V(24);
      BAR();
      issue(t + 5, slot);
      slot = (slot == K2_NS - 1) ? 0 : slot + 1;
    }
    WAIT_V(24); BAR();
    WAIT_V(16); BAR();
    WAIT_V(8);  BAR();
    WAIT_V(0);  BAR();
    return;
  }
  const int q = lane >> 4, i16 = lane & 15;
  u32x2 z1v[2][7];
  float bs[2];
  if (c < 8) {
#pragma unroll
    for (int m = 0; m < 2; ++m) {
      load_pairs(ws + OFF_Z1 + (size_t)(8 * r + 2 * wid + m) * (28 * 1024) + lane * 16, 7 * c, z1v[m]);
      bs[m] = bf2f(bsp[128 * r + 32 * wid + 8 * (i16 >> 2) + 4 * m + (i16 & 3)]);
    }
  }
  f32x4 acc[2][7];
#pragma unroll
  for (int m = 0; m < 2; ++m)
#pragma unroll
    for (int j = 0; j < 7; ++j) acc[m][j] = f32x4{0.f, 0.f, 0.f, 0.f};
  const int a0 = (32 * wid + i16) * 128 + ((q ^ ((i16 >> 1) & 7)) << 4), a1 = a0 ^ 64;
  const int b0 = 16384 + lane * 16;
  bf16x8 wf0[2], nf0[7], wf1[2], nf1[7];
  BAR();
  {
    const char* sl = smem;
    wf0[0] = ldsr(sl + a0); wf0[1] = ldsr(sl + a0 + 2048);
#pragma unroll
    for (int j = 0; j < 7; ++j) nf0[j] = ldsr(sl + b0 + j * 1024);
  }
  int so = 0;
  for (int t = 0; t < 32; ++t) {
    {
      const char* sl = smem + so;
      wf1[0] = ldsr(sl + a1); wf1[1] = ldsr(sl + a1 + 2048);
#pragma unroll
      for (int j = 0; j < 7; ++j) nf1[j] = ldsr(sl + b0 + (7 + j) * 1024);
    }
    SCHED();
#pragma unroll
    for (int j = 0; j < 7; ++j) {
      acc[0][j] = MFMA(nf0[j], wf0[0], acc[0][j]);
      acc[1][j] = MFMA(nf0[j], wf0[1], acc[1][j]);
    }
    SCHED();
    so = (so == (K2_NS - 1) * K2_SB) ? 0 : so + K2_SB;
    if (t < 31) {
      WAIT_L0();
      BAR();
      const char* sl = smem + so;
      wf0[0] = ldsr(sl + a0); wf0[1] = ldsr(sl + a0 + 2048);
#pragma unroll
      for (int j = 0; j < 7; ++j) nf0[j] = ldsr(sl + b0 + j * 1024);
    }
    SCHED();
#pragma unroll
    for (int j = 0; j < 7; ++j) {
      acc[0][j] = MFMA(nf1[j], wf1[0], acc[0][j]);
      acc[1][j] = MFMA(nf1[j], wf1[1], acc[1][j]);
    }
    SCHED();
  }
#pragma unroll
  for (int m = 0; m < 2; ++m) {
    u32x2 v[7];
    if (c < 8) {
#pragma unroll
      for (int j = 0; j < 7; ++j)
        v[j] = u32x2{pk2(blo(z1v[m][j].x) * (acc[m][j][0] + bs[m]), bhi(z1v[m][j].x) * (acc[m][j][1] + bs[m])),
                     pk2(blo(z1v[m][j].y) * (acc[m][j][2] + bs[m]), bhi(z1v[m][j].y) * (acc[m][j][3] + bs[m]))};
    } else {
#pragma unroll
      for (int j = 0; j < 7; ++j) v[j] = u32x2{pk2(acc[m][j][0], acc[m][j][1]), pk2(acc[m][j][2], acc[m][j][3])};
    }
    store_pairs(ws + OFF_OMAT + (size_t)(8 * r + 2 * wid + m) * (56 * 1024) + lane * 16, 7 * c, v);
  }
}

constexpr int K3_LDS = 114688 + 16384;
__global__ __launch_bounds__(512, 2) void k3(const char* __restrict__ wout, const char* ws, char* __restrict__ out) {
  __shared__ __attribute__((aligned(1024))) char smem[K3_LDS];
  const int b = blockIdx.x, xcd = b & 7, idx = b >> 3;
  const int rb = xcd * 4 + idx / 7, db = idx % 7;
  const int tid = threadIdx.x, wid = __builtin_amdgcn_readfirstlane(tid >> 6), lane = tid & 63;
  const int q = lane >> 4, i16 = lane & 15;
  const int tq = wid & 3, kh = wid >> 2;
  const int T = 4 * rb + tq;
  {
    const int rl = lane >> 3;
    const u32 off = (u32)((8 * wid + rl) * 896 + db * 128 + (((lane & 7) ^ (((rl >> 1) & 3) << 1)) << 4));
#pragma unroll
    for (int k = 0; k < 14; ++k) GLDS(wout + (size_t)k * (64 * 896) + off, smem + (wid + 8 * k) * 1024);
  }
  const char* om = ws + OFF_OMAT + (size_t)T * (56 * 1024) + lane * 16;
  bf16x8 hf[14];
#pragma unroll
  for (int p = 0; p < 14; ++p) hf[p] = *(const bf16x8*)(om + (14 * kh + p) * 1024);
  const u32x4 yr = *(const u32x4*)(om + (28 + 2 * db + kh) * 1024);
  const u32x4 yi = *(const u32x4*)(om + (42 + 2 * db + kh) * 1024);
  const int trow = 32 * (T >> 1) + 8 * (i16 >> 2) + 4 * (T & 1) + (i16 & 3);
  const float gate = ((const float*)(ws + OFF_GATE))[trow];
  f32x4 acc[4];
#pragma unroll
  for (int j = 0; j < 4; ++j) acc[j] = f32x4{0.f, 0.f, 0.f, 0.f};
  WAIT_V(0);
  BAR();
  const int a = i16 >> 2, pp = lane & 3;
  const int rloc = 4 * q + a;
  const int sw = ((rloc >> 1) & 3) << 1;
  const char* tb = smem + (size_t)(448 * kh + rloc) * 128 + (pp & 1) * 8;
  int co[4];
#pragma unroll
  for (int j = 0; j < 4; ++j) co[j] = ((2 * j + (pp >> 1)) ^ sw) << 4;
#pragma unroll
  for (int p = 0; p < 14; ++p) {
#pragma unroll
    for (int j = 0; j < 4; ++j) {
      const bf16x8 wf = lds_tr2(tb + co[j] + p * 4096, tb + co[j] + p * 4096 + 2048);
      acc[j] = MFMA(wf, hf[p], acc[j]);
    }
  }
  LAS f32x4* red = (LAS f32x4*)(LAS char*)(smem + 114688);
  red[(wid * 2 + 0) * 64 + lane] = kh ? acc[0] : acc[2];
  red[(wid * 2 + 1) * 64 + lane] = kh ? acc[1] : acc[3];
  WAIT_L0();
  BAR();
  const f32x4 o0 = (kh ? acc[2] : acc[0]) + red[((wid ^ 4) * 2 + 0) * 64 + lane];
  const f32x4 o1 = (kh ? acc[3] : acc[1]) + red[((wid ^ 4) * 2 + 1) * 64 + lane];
  const float og = 1.0f - gate;
  char* op = out + (size_t)trow * 896 + db * 128 + kh * 64 + q * 8;
  *(u32x2*)(op) = u32x2{pk2(gate * (blo(yr.x) - blo(yi.x)) + og * o0[0], gate * (bhi(yr.x) - bhi(yi.x)) + og * o0[1]),
                        pk2(gate * (blo(yr.y) - blo(yi.y)) + og * o0[2], gate * (bhi(yr.y) - bhi(yi.y)) + og * o0[3])};
  *(u32x2*)(op + 32) = u32x2{pk2(gate * (blo(yr.z) - blo(yi.z)) + og * o1[0], gate * (bhi(yr.z) - bhi(yi.z)) + og * o1[1]),
                             pk2(gate * (blo(yr.w) - blo(yi.w)) + og * o1[2], gate * (bhi(yr.w) - bhi(yi.w)) + og * o1[3])};
}

extern "C" void kernel_launch(void* const* d_in, const int* in_sizes, int n_in, void* d_out, int out_size, void* d_ws, size_t ws_size,
                              hipStream_t stream) {
  const char* x = (const char*)d_in[0];
  const char* wsr = (const char*)d_in[1];
  const char* wsi = (const char*)d_in[2];
  const char* wdr = (const char*)d_in[3];
  const char* wdi = (const char*)d_in[4];
  const char* win = (const char*)d_in[6];
  const char* wsp = (const char*)d_in[7];
  const u16* bsp = (const u16*)d_in[8];
  const char* wout = (const char*)d_in[9];
  const char* gw = (const char*)d_in[10];
  char* ws = (char*)d_ws;
  k1<<<256, 512, 0, stream>>>(x, win, wdr, wdi, gw, ws);
  k2<<<256, 512, 0, stream>>>(wsp, wsr, wsi, bsp, ws);
  k3<<<224, 512, 0, stream>>>(wout, ws, (char*)d_out);
}
```

```cpp
#include <hip/hip_runtime.h>
#include <hip/hip_bf16.h>

#define LAS __attribute__((address_space(3)))
#define GAS __attribute__((address_space(1)))
typedef unsigned short u16;
typedef unsigned int u32;
typedef unsigned long long u64;
typedef short bf16x8 __attribute__((ext_vector_type(8)));
typedef short bf16x4 __attribute__((ext_vector_type(4)));
typedef float f32x4 __attribute__((ext_vector_type(4)));
typedef float f32x2 __attribute__((ext_vector_type(2)));
typedef unsigned u32x4 __attribute__((ext_vector_type(4)));
typedef unsigned u32x2 __attribute__((ext_vector_type(2)));
typedef __bf16 bf2_t __attribute__((ext_vector_type(2)));

#define DI __device__ __forceinline__
#define MFMA(a, b, c) __builtin_amdgcn_mfma_f32_16x16x32_bf16((a), (b), (c), 0, 0, 0)
#define WAIT_V(n) asm volatile("s_waitcnt vmcnt(%0)" ::"i"(n) : "memory")
#define WAIT_L0() asm volatile("s_waitcnt lgkmcnt(0)" ::: "memory")
#define BAR() do { asm volatile("" ::: "memory"); __builtin_amdgcn_s_barrier(); asm volatile("" ::: "memory"); } while (0)
#define SCHED() __builtin_amdgcn_sched_barrier(0)
#define SGB(mask, n) __builtin_amdgcn_sched_group_barrier(mask, n, 0)
#define RLX_AGENT __ATOMIC_RELAXED, __HIP_MEMORY_SCOPE_AGENT

#define PF_SP 16
#define PF_Y 8

constexpr size_t BMAT_C = 458752;
constexpr size_t OFF_BMAT = 0;
constexpr size_t OFF_Z1 = OFF_BMAT + 16 * BMAT_C;
constexpr size_t OFF_OMAT = OFF_Z1 + 128 * 28 * 1024;
constexpr size_t OFF_GATE = OFF_OMAT + 128 * 56 * 1024;
constexpr size_t OFF_XCH = OFF_GATE + 2048 * 4;
constexpr size_t OFF_F1 = OFF_XCH + 16 * 8 * 128 * 16;
constexpr size_t OFF_F1Z = OFF_F1 + 16 * 128;
constexpr size_t OFF_F2 = OFF_F1Z + 16 * 128;
constexpr size_t OFF_EPOCH = OFF_F2 + 16 * 128;
constexpr size_t WS_END = OFF_EPOCH + 128;
constexpr u32 WS_BYTES = (u32)WS_END;

constexpr size_t OFF_DBG = 64ull << 20;
#define STAMP(id) do {} while (0)
#define CSTAMP(id) do {} while (0)

DI float bf2f(u16 v) { return __uint_as_float(((u32)v) << 16); }
DI u32 pk2(float lo, float hi) { bf2_t v = {(__bf16)lo, (__bf16)hi}; return __builtin_bit_cast(u32, v); }
DI float blo(u32 v) { return __uint_as_float(v << 16); }
DI float bhi(u32 v) { return __uint_as_float(v & 0xffff0000u); }
DI f32x4 gelu4(f32x4 x) {
  const f32x4 a = x * (x * x * -0.1029432397f + -2.302208198f);
  f32x4 d;
#pragma unroll
  for (int e = 0; e < 4; ++e) d[e] = __builtin_amdgcn_exp2f(a[e]);
  d = d + 1.0f;
#pragma unroll
  for (int e = 0; e < 4; ++e) d[e] = __builtin_amdgcn_rcpf(d[e]);
  return x * d;
}
DI int rowperm(int rho) { return 32 * (rho >> 5) + 8 * ((rho & 15) >> 2) + 4 * ((rho >> 4) & 1) + (rho & 3); }
DI int swap23(int v) { return (v & ~12) | ((v & 4) << 1) | ((v & 8) >> 1); }
template <int CTRL> DI float dpp_add(float v) { return v + __builtin_bit_cast(float, __builtin_amdgcn_update_dpp(0, __builtin_bit_cast(int, v), CTRL, 0xf, 0xf, false)); }
template <int CTRL> DI void dpp_add8(f32x4& a, f32x4& b) {
#pragma unroll
  for (int e = 0; e < 4; ++e) { a[e] = dpp_add<CTRL>(a[e]); b[e] = dpp_add<CTRL>(b[e]); }
}
DI void row16_sum8(f32x4& a, f32x4& b) { dpp_add8<0x128>(a, b); dpp_add8<0x124>(a, b); dpp_add8<0x122>(a, b); dpp_add8<0x121>(a, b); }
DI float quad_sum(float v) { return dpp_add<0x4E>(dpp_add<0xB1>(v)); }
DI bf16x8 ldsr(const char* p) { return *(const LAS bf16x8*)(const LAS char*)p; }
DI bf16x8 lds_tr2(const char* p0, const char* p1) {
  bf16x4 a = __builtin_amdgcn_ds_read_tr16_b64_v4i16((LAS bf16x4*)(LAS char*)p0);
  bf16x4 b = __builtin_amdgcn_ds_read_tr16_b64_v4i16((LAS bf16x4*)(LAS char*)p1);
  return __builtin_shufflevector(a, b, 0, 1, 2, 3, 4, 5, 6, 7);
}
DI void glds(const char* base, u32 voff, u32 lds) {
  asm volatile("s_mov_b32 m0, %2\n\ts_nop 0\n\tglobal_load_lds_dwordx4 %0, %1" ::"v"(voff), "s"(base), "s"(lds) : "memory");
}
DI void glds_sc1(const char* base, u32 voff, u32 lds) {
  asm volatile("s_mov_b32 m0, %2\n\ts_nop 0\n\tglobal_load_lds_dwordx4 %0, %1 sc1" ::"v"(voff), "s"(base), "s"(lds) : "memory");
}
DI void touch(const char* base, u32 voff, u32 lds) {
  asm volatile("s_mov_b32 m0, %2\n\ts_nop 0\n\tglobal_load_lds_dword %0, %1" ::"v"(voff), "s"(base), "s"(lds) : "memory");
}
typedef __amdgpu_buffer_rsrc_t rsrc_t;
DI void st16(rsrc_t rs, u32 off, u32x4 v) { __builtin_amdgcn_raw_buffer_store_b128(v, rs, off, 0, 16); }
DI void st8(rsrc_t rs, u32 off, u32x2 v) { __builtin_amdgcn_raw_buffer_store_b64(v, rs, off, 0, 16); }
DI u32x4 ld16(rsrc_t rs, u32 off) { return __builtin_amdgcn_raw_buffer_load_b128(rs, off, 0, 16); }
DI u32 ld4(rsrc_t rs, u32 off) { return __builtin_amdgcn_raw_buffer_load_b32(rs, off, 0, 16); }

DI void store_pairs(rsrc_t rs, u32 off, int c7, const u32x2 (&v)[7]) {
  const u32 p = off + (u32)(c7 >> 1) * 1024;
  if ((c7 & 1) == 0) {
#pragma unroll
    for (int k = 0; k < 3; ++k) st16(rs, p + k * 1024, u32x4{v[2 * k].x, v[2 * k].y, v[2 * k + 1].x, v[2 * k + 1].y});
    st8(rs, p + 3 * 1024, v[6]);
  } else {
    st8(rs, p + 8, v[0]);
#pragma unroll
    for (int k = 0; k < 3; ++k) st16(rs, p + (k + 1) * 1024, u32x4{v[2 * k + 1].x, v[2 * k + 1].y, v[2 * k + 2].x, v[2 * k + 2].y});
  }
}
DI void wait_flags(rsrc_t rs, u32 off, int n, u32 tag, int lane) {
  const u64 t0 = __builtin_amdgcn_s_memrealtime();
  const u32 o = off + (lane < n ? lane : 0) * 4;
  for (;;) {
    const u32 v = ld4(rs, o);
    if (__all(v == tag)) break;
    if (__builtin_amdgcn_s_memrealtime() - t0 > 2000000ull) break;
    __builtin_amdgcn_s_sleep(1);
  }
}

constexpr int SB2 = 30720, NS2 = 5;
constexpr int L_GW = 153600, L_PART = L_GW + 1024, L_FIN = L_PART + 2048, L_DUMMY = L_FIN + 1024, LDS_BYTES = L_DUMMY + 2048;

__global__ __launch_bounds__(512, 2) void fused(const char* __restrict__ x, const char* __restrict__ win, const char* __restrict__ wdr,
                                                const char* __restrict__ wdi, const char* __restrict__ gw, const char* __restrict__ wsp,
                                                const char* __restrict__ wsr, const char* __restrict__ wsi, const u16* __restrict__ bsp,
                                                const char* __restrict__ wout, char* ws, char* __restrict__ out) {
  __shared__ __attribute__((aligned(1024))) char smem[LDS_BYTES];
  STAMP(0);
  const u32 lb = (u32)(size_t)(LAS char*)smem;
  const int xcd = blockIdx.x & 7, idx = blockIdx.x >> 3;
  int r, c;
  if ((xcd & 1) == 0) { r = 4 * (xcd >> 1) + (idx >> 3); c = idx & 7; }
  else if (xcd < 4) { r = 8 * (xcd >> 1) + (idx >> 2); c = 8 + (idx & 3); }
  else { r = 8 * ((xcd >> 1) - 2) + (idx >> 2); c = 12 + (idx & 3); }
  const int tid = threadIdx.x, wid = __builtin_amdgcn_readfirstlane(tid >> 6), lane = tid & 63;
  const int q = lane >> 4, i16 = lane & 15;
  const rsrc_t rs = __builtin_amdgcn_make_buffer_rsrc(ws, 0, WS_BYTES, 0x00020000);
  u32 tag = ld4(rs, (u32)OFF_EPOCH);
  const char* W = (c < 8) ? wsp : (c < 12) ? wsr : wsi;
  const int lw = wid & 3;
  u32 aoff[4];
#pragma unroll
  for (int k = 0; k < 4; ++k) { const int rho = 8 * (4 * lw + k) + (lane >> 3); aoff[k] = (u32)((128 * r + rowperm(rho)) * 4096 + (((lane & 7) ^ ((rho >> 1) & 7)) << 4)); }

  {
    u32 xoff[2];
#pragma unroll
    for (int k = 0; k < 2; ++k) { const int rho = 8 * (2 * wid + k) + (lane >> 3); xoff[k] = (u32)((128 * r + rowperm(rho)) * 896 + (((lane & 7) ^ ((rho >> 1) & 7)) << 4)); }
    const int zc = (c < 8) ? (896 + 112 * c) : 112 * (c - 8);
    u32 woff[2];
#pragma unroll
    for (int k = 0; k < 2; ++k) { int P = 112 * wid + 64 * k + lane; if (k == 1 && lane >= 48) P = 112 * wid; const int rho = P / 14, ch = P - rho * 14; woff[k] = (u32)(swap23(rho) * 3584 + zc * 2 + ch * 16); }
    const int mw = wid & 3;
    const int a0 = (32 * mw + i16) * 128 + ((q ^ ((i16 >> 1) & 7)) << 4), a1 = a0 ^ 64;
    const int trb = (16 * (q >> 1) + 4 * (q & 1) + (i16 >> 2)) * 224 + (lane & 3) * 8;
    int k0 = (c + 2 * r) % 7;
    auto kof = [&](int s) { int k = k0 + s; return k >= 7 ? k - 7 : k; };

    if (c < 8) {
      constexpr int SB = 30720, NS = 5;
      auto issue = [&](int s) {
        const u32 sl = lb + (s % NS) * SB; const int k = kof(s);
        glds(x + k * 128, xoff[0], sl + (2 * wid) * 1024);
        glds(x + k * 128, xoff[1], sl + (2 * wid + 1) * 1024);
        glds(win + (size_t)k * (64 * 3584), woff[0], sl + 16384 + wid * 1792);
        if (lane < 48) glds(win + (size_t)k * (64 * 3584), woff[1], sl + 16384 + wid * 1792 + 1024);
      };
      STAMP(14);
#pragma unroll
      for (int s = 0; s < NS; ++s) issue(s);
      STAMP(15);
      const int nh = wid >> 2;
      f32x4 acc[2][4];
#pragma unroll
      for (int m = 0; m < 2; ++m)
#pragma unroll
        for (int j = 0; j < 4; ++j) acc[m][j] = f32x4{0.f, 0.f, 0.f, 0.f};
#pragma unroll
      for (int s = 0; s < 7; ++s) {
        const int issued = (NS + (s > 1 ? s - 1 : 0)) > 7 ? 7 : (NS + (s > 1 ? s - 1 : 0));
        WAIT_V(4 * (issued - s - 1));
        WAIT_L0();
        BAR();
        if (s == 0) STAMP(1);
        if (s >= 1 && s - 1 + NS < 7) issue(s - 1 + NS);
        const char* sl = smem + (s % NS) * SB;
        const char* tw = sl + 16384 + trb + nh * 128;
#pragma unroll
        for (int kb = 0; kb < 2; ++kb) {
          const bf16x8 xf0 = ldsr(sl + (kb ? a1 : a0)), xf1 = ldsr(sl + (kb ? a1 : a0) + 2048);
#pragma unroll
          for (int j = 0; j < 4; ++j) {
            if (j < 3 || nh == 0) {
              const bf16x8 wf = lds_tr2(tw + (32 * kb) * 224 + 32 * j, tw + (32 * kb + 8) * 224 + 32 * j);
              acc[0][j] = MFMA(xf0, wf, acc[0][j]);
              acc[1][j] = MFMA(xf1, wf, acc[1][j]);
            }
          }
        }
      }
      asm volatile("" : "+v"(tag));
      tag += 1u;
      STAMP(2);
      LAS f32x2* part = (LAS f32x2*)(LAS char*)(smem + L_PART);
      LAS f32x2* fin = (LAS f32x2*)(LAS char*)(smem + L_FIN);
      {
#pragma unroll
        for (int j = 0; j < 4; ++j) if (j < 3 || nh == 0) { acc[0][j] = gelu4(acc[0][j]); acc[1][j] = gelu4(acc[1][j]); }
        f32x4 s0 = (acc[0][0] + acc[0][1]) + (acc[0][2] + acc[0][3]), s1 = (acc[1][0] + acc[1][1]) + (acc[1][2] + acc[1][3]);
        row16_sum8(s0, s1);
        const float icnt = nh ? (1.f / 48.f) : (1.f / 64.f);
        s0 *= icnt; s1 *= icnt;
        f32x4 v0 = {0.f, 0.f, 0.f, 0.f}, v1 = {0.f, 0.f, 0.f, 0.f};
#pragma unroll
        for (int j = 0; j < 4; ++j) if (j < 3 || nh == 0) { const f32x4 d0 = acc[0][j] - s0, d1 = acc[1][j] - s1; v0 += d0 * d0; v1 += d1 * d1; }
        row16_sum8(v0, v1);
        if (i16 == 0) {
#pragma unroll
          for (int e = 0; e < 4; ++e) {
            part[(32 * mw + 8 * q + e) * 2 + nh] = f32x2{s0[e], v0[e]};
            part[(32 * mw + 8 * q + 4 + e) * 2 + nh] = f32x2{s1[e], v1[e]};
          }
        }
      }
      WAIT_L0();
      BAR();
      STAMP(3);
      const u32 xch = (u32)OFF_XCH + r * (8 * 128 * 16);
      if (tid < 128) {
        const f32x2 pa = part[tid * 2], pb = part[tid * 2 + 1];
        const float mean = (pa.x * 64.f + pb.x * 48.f) * (1.f / 112.f);
        const float dl = pa.x - pb.x;
        const float m2 = pa.y + pb.y + dl * dl * (64.f * 48.f / 112.f);
        st16(rs, xch + c * 2048 + tid * 16, u32x4{__float_as_uint(mean), tag, __float_as_uint(m2), tag});
      }
      {
        const int row = tid >> 2, g = tid & 3;
        u32x4 ga, gb;
        const u64 t0 = __builtin_amdgcn_s_memrealtime();
        for (;;) {
          ga = ld16(rs, xch + (2 * g) * 2048 + row * 16);
          gb = ld16(rs, xch + (2 * g + 1) * 2048 + row * 16);
          const bool ok = ga.y == tag && ga.w == tag && gb.y == tag && gb.w == tag;
          if (__all(ok)) break;
          if (__builtin_amdgcn_s_memrealtime() - t0 > 2000000ull) break;
          __builtin_amdgcn_s_sleep(1);
        }
        const float ma = __uint_as_float(ga.x), mb = __uint_as_float(gb.x);
        const float mean = quad_sum(ma + mb) * 0.125f;
        const float da = ma - mean, db = mb - mean;
        const float m2 = quad_sum(__uint_as_float(ga.z) + __uint_as_float(gb.z) + 112.f * (da * da + db * db));
        if (g == 0) fin[row] = f32x2{mean, __builtin_amdgcn_rsqf(m2 * (1.f / 896.f) + 1e-5f)};
      }
      WAIT_L0();
      BAR();
      STAMP(4);
      f32x2 st[8];
#pragma unroll
      for (int e = 0; e < 8; ++e) st[e] = fin[32 * mw + 8 * q + e];
      const u32 bo = (u32)OFF_BMAT + c * (u32)BMAT_C + ((4 * r + mw) * 7 + 4 * nh) * 1024 + lane * 16;
#pragma unroll
      for (int j = 0; j < 4; ++j) {
        if (j < 3 || nh == 0) {
          float v[8];
#pragma unroll
          for (int e = 0; e < 8; ++e) v[e] = (acc[e >> 2][j][e & 3] - st[e].x) * st[e].y;
          st16(rs, bo + j * 1024, u32x4{pk2(v[0], v[1]), pk2(v[2], v[3]), pk2(v[4], v[5]), pk2(v[6], v[7])});
        }
      }
      if (wid >= 4) {
#pragma unroll
        for (int s = 0; s < NS2; ++s)
#pragma unroll
          for (int k = 0; k < 4; ++k) glds(W + s * 128, aoff[k], lb + s * SB2 + (4 * lw + k) * 1024);
        WAIT_V(20);
      } else {
        WAIT_V(0);
      }
      BAR();
      if (tid == 0) __hip_atomic_store((GAS u32*)(ws + OFF_F1 + c * 128 + r * 4), tag, RLX_AGENT);
      STAMP(5);
    } else {
      constexpr int SB = 45056, NS = 3;
      const char* wd = (c < 12) ? wdr : wdi;
      u32 doff[2];
#pragma unroll
      for (int k = 0; k < 2; ++k) { int P = 112 * wid + 64 * k + lane; if (k == 1 && lane >= 48) P = 112 * wid; const int e = P >> 3; doff[k] = (u32)((112 * (c & 3) + e) * 896 + (((P & 7) ^ ((e >> 1) & 7)) << 4)); }
      if (wid == 0 && lane < 56) glds(gw, lane * 16, lb + L_GW);
      auto issue = [&](int s) {
        const u32 sl = lb + (s % NS) * SB; const int k = kof(s);
        glds(x + k * 128, xoff[0], sl + (2 * wid) * 1024);
        glds(x + k * 128, xoff[1], sl + (2 * wid + 1) * 1024);
        glds(wd + k * 128, doff[0], sl + 16384 + wid * 1792);
        glds(win + (size_t)k * (64 * 3584), woff[0], sl + 30720 + wid * 1792);
        if (lane < 48) {
          glds(wd + k * 128, doff[1], sl + 16384 + wid * 1792 + 1024);
          glds(win + (size_t)k * (64 * 3584), woff[1], sl + 30720 + wid * 1792 + 1024);
        }
      };
      STAMP(14);
#pragma unroll
      for (int s = 0; s < NS; ++s) issue(s);
      STAMP(15);
      f32x4 acc[2][7], gacc[2];
#pragma unroll
      for (int m = 0; m < 2; ++m) {
        gacc[m] = f32x4{0.f, 0.f, 0.f, 0.f};
#pragma unroll
        for (int j = 0; j < 7; ++j) acc[m][j] = f32x4{0.f, 0.f, 0.f, 0.f};
      }
      const int d0 = i16 * 128 + ((q ^ ((i16 >> 1) & 7)) << 4);
#pragma unroll
      for (int s = 0; s < 7; ++s) {
        const int issued = (NS + (s > 1 ? s - 1 : 0)) > 7 ? 7 : (NS + (s > 1 ? s - 1 : 0));
        WAIT_V(6 * (issued - s - 1));
        WAIT_L0();
        BAR();
        if (s == 0) STAMP(1);
        if (s >= 1 && s - 1 + NS < 7) issue(s - 1 + NS);
        const char* sl = smem + (s % NS) * SB;
        if (wid < 4) {
#pragma unroll
          for (int kb = 0; kb < 2; ++kb) {
            const bf16x8 xf0 = ldsr(sl + (kb ? a1 : a0)), xf1 = ldsr(sl + (kb ? a1 : a0) + 2048);
#pragma unroll
            for (int j = 0; j < 7; ++j) {
              const bf16x8 wf = ldsr(sl + 16384 + (kb ? (d0 ^ 64) : d0) + j * 2048);
              acc[0][j] = MFMA(xf0, wf, acc[0][j]);
              acc[1][j] = MFMA(xf1, wf, acc[1][j]);
            }
          }
        } else {
          const char* tw = sl + 30720 + trb;
#pragma unroll
          for (int kb = 0; kb < 2; ++kb) {
            const bf16x8 xf0 = ldsr(sl + (kb ? a1 : a0)), xf1 = ldsr(sl + (kb ? a1 : a0) + 2048);
            const bf16x8 gf = ldsr(smem + L_GW + kof(s) * 128 + kb * 64 + q * 16);
            gacc[0] = MFMA(xf0, gf, gacc[0]);
            gacc[1] = MFMA(xf1, gf, gacc[1]);
#pragma unroll
            for (int j = 0; j < 7; ++j) {
              const bf16x8 wf = lds_tr2(tw + (32 * kb) * 224 + 32 * j, tw + (32 * kb + 8) * 224 + 32 * j);
              acc[0][j] = MFMA(wf, xf0, acc[0][j]);
              acc[1][j] = MFMA(wf, xf1, acc[1][j]);
            }
          }
        }
      }
      asm volatile("" : "+v"(tag));
      tag += 1u;
      STAMP(2);
      if (wid < 4) {
        const u32 bo = (u32)OFF_BMAT + c * (u32)BMAT_C + ((4 * r + mw) * 7) * 1024 + lane * 16;
#pragma unroll
        for (int j = 0; j < 7; ++j)
          st16(rs, bo + j * 1024, u32x4{pk2(acc[0][j][0], acc[0][j][1]), pk2(acc[0][j][2], acc[0][j][3]), pk2(acc[1][j][0], acc[1][j][1]), pk2(acc[1][j][2], acc[1][j][3])});
        WAIT_V(0);
      }
      WAIT_L0();
      BAR();
      if (tid == 0) __hip_atomic_store((GAS u32*)(ws + OFF_F1 + c * 128 + r * 4), tag, RLX_AGENT);
      STAMP(3);
      if (wid < 4) {
#pragma unroll
        for (int l0 = 0; l0 < PF_SP; l0 += 16) touch(wsp, (u32)((128 * r + 16 * (c - 8) + i16) * 4096 + (l0 + 4 * wid + q) * 128), lb + L_DUMMY + wid * 256);
#pragma unroll
        for (int l0 = 0; l0 < PF_Y; l0 += 8) touch(W, (u32)((128 * r + 32 * (c & 3) + (lane & 31)) * 4096 + (l0 + 2 * wid + (lane >> 5)) * 128), lb + L_DUMMY + wid * 256);
      } else {
#pragma unroll
        for (int m = 0; m < 2; ++m) {
          u32x2 v[7];
#pragma unroll
          for (int j = 0; j < 7; ++j) { const f32x4 g = gelu4(acc[m][j]); v[j] = u32x2{pk2(g[0], g[1]), pk2(g[2], g[3])}; }
          store_pairs(rs, (u32)OFF_Z1 + (8 * r + 2 * mw + m) * (28 * 1024) + lane * 16, 7 * (c - 8), v);
        }
        if (c == 8 && i16 == 0) {
          u32x4 g0, g1;
#pragma unroll
          for (int e = 0; e < 4; ++e) {
            g0[e] = __float_as_uint(__builtin_amdgcn_rcpf(1.0f + __builtin_amdgcn_exp2f(-1.442695041f * gacc[0][e])));
            g1[e] = __float_as_uint(__builtin_amdgcn_rcpf(1.0f + __builtin_amdgcn_exp2f(-1.442695041f * gacc[1][e])));
          }
          const u32 gp = (u32)OFF_GATE + (128 * r + 32 * mw + 8 * q) * 4;
          st16(rs, gp, g0); st16(rs, gp + 16, g1);
        }
#pragma unroll
        for (int s = 0; s < NS2; ++s)
#pragma unroll
          for (int k = 0; k < 4; ++k) glds(W + s * 128, aoff[k], lb + s * SB2 + (4 * lw + k) * 1024);
        WAIT_V(20);
      }
      BAR();
      if (tid == 0) __hip_atomic_store((GAS u32*)(ws + OFF_F1Z + r * 128 + (c - 8) * 4), tag, RLX_AGENT);
      STAMP(5);
    }
  }

  const bool p3 = idx < 28;
  const int rb = xcd * 4 + idx / 7, db = idx % 7;
  if (wid >= 4) {
    if (wid == 4) wait_flags(rs, (u32)OFF_F1 + c * 128, 16, tag, lane);
    BAR();
    STAMP(6);
    const char* Bm = ws + OFF_BMAT + (size_t)c * BMAT_C + lw * 3584;
    const u32 boff = lane * 16;
    auto issueB = [&](int s, int slot) {
      const u32 sl = lb + slot * SB2 + 16384 + lw * 3584;
      const char* wb = Bm + (size_t)s * 14336;
#pragma unroll
      for (int k = 0; k < 3; ++k) glds_sc1(wb + k * 1024, boff, sl + k * 1024);
      if (lane < 32) glds_sc1(wb + 3072, boff, sl + 3072);
    };
    auto issueA = [&](int s, int slot) {
#pragma unroll
      for (int k = 0; k < 4; ++k) glds(W + s * 128, aoff[k], lb + slot * SB2 + (4 * lw + k) * 1024);
    };
#pragma unroll
    for (int s = 0; s < NS2; ++s) issueB(s, s);
    WAIT_V(16); BAR();
    WAIT_V(12); BAR(); issueA(5, 0); issueB(5, 0);
    WAIT_V(16); BAR(); issueA(6, 1); issueB(6, 1);
    WAIT_V(20); BAR(); issueA(7, 2); issueB(7, 2);
    int slot = 3;
#pragma unroll 1
    for (int t = 3; t < 27; ++t) {
      WAIT_V(24);
      BAR();
      issueA(t + 5, slot); issueB(t + 5, slot);
      slot = (slot == NS2 - 1) ? 0 : slot + 1;
    }
    const int rl = lane >> 3;
    const u32 wo = (u32)((56 * lw + rl) * 896 + db * 128 + (((lane & 7) ^ (((rl >> 1) & 3) << 1)) << 4));
    auto issueW = [&](int i, int slot_) {
      if (p3) {
#pragma unroll
        for (int k = 0; k < 7; ++k) glds(wout + (size_t)(224 * i + 8 * k) * 896, wo, lb + slot_ * SB2 + (7 * lw + k) * 1024);
      }
    };
    if (p3) {
      WAIT_V(24); BAR(); issueW(0, 2);
      WAIT_V(23); BAR(); issueW(1, 3);
      WAIT_V(22); BAR(); issueW(2, 4);
      WAIT_V(21); BAR(); issueW(3, 0);
    } else {
      WAIT_V(24); BAR();
      WAIT_V(16); BAR();
      WAIT_V(8);  BAR();
      WAIT_V(0);  BAR();
    }
  } else {
    f32x4 acc[2][7];
#pragma unroll
    for (int m = 0; m < 2; ++m)
#pragma unroll
      for (int j = 0; j < 7; ++j) acc[m][j] = f32x4{0.f, 0.f, 0.f, 0.f};
    const int a0 = (32 * wid + i16) * 128 + ((q ^ ((i16 >> 1) & 7)) << 4), a1 = a0 ^ 64;
    const int b0 = 16384 + lane * 16;
    bf16x8 wf0[2], nf0[7], wf1[2], nf1[7];
    BAR();
    BAR();
    STAMP(7);
    CSTAMP(16);
    {
      const char* sl = smem;
      wf0[0] = ldsr(sl + a0); wf0[1] = ldsr(sl + a0 + 2048);
#pragma unroll
      for (int j = 0; j < 7; ++j) nf0[j] = ldsr(sl + b0 + j * 1024);
    }
    SCHED();
    int so = 0;
#define INTERLEAVE_A() do { SGB(0x8, 1); SGB(0x100, 2); SGB(0x8, 1); SGB(0x100, 1); SGB(0x8, 1); SGB(0x100, 1); SGB(0x8, 1); SGB(0x100, 1); SGB(0x8, 1); SGB(0x100, 1); \
    SGB(0x8, 1); SGB(0x100, 1); SGB(0x8, 1); SGB(0x100, 1); SGB(0x8, 1); SGB(0x100, 1); SGB(0x8, 6); } while (0)
    for (int t = 0; t < 32; ++t) {
      {
        const char* sl = smem + so;
        wf1[0] = ldsr(sl + a1); wf1[1] = ldsr(sl + a1 + 2048);
#pragma unroll
        for (int j = 0; j < 7; ++j) nf1[j] = ldsr(sl + b0 + (7 + j) * 1024);
      }
#pragma unroll
      for (int j = 0; j < 7; ++j) {
        acc[0][j] = MFMA(nf0[j], wf0[0], acc[0][j]);
        acc[1][j] = MFMA(nf0[j], wf0[1], acc[1][j]);
      }
      INTERLEAVE_A();
      SCHED();
      so = (so == (NS2 - 1) * SB2) ? 0 : so + SB2;
      if (t < 31) {
        WAIT_L0();
        BAR();
      }
      SCHED();
      {
        const char* sl = smem + so;
        wf0[0] = ldsr(sl + a0); wf0[1] = ldsr(sl + a0 + 2048);
#pragma unroll
        for (int j = 0; j < 7; ++j) nf0[j] = ldsr(sl + b0 + j * 1024);
      }
#pragma unroll
      for (int j = 0; j < 7; ++j) {
        acc[0][j] = MFMA(nf1[j], wf1[0], acc[0][j]);
        acc[1][j] = MFMA(nf1[j], wf1[1], acc[1][j]);
      }
      INTERLEAVE_A();
      SCHED();
    }
    CSTAMP(17);
    STAMP(8);
    if (c < 8) {
      wait_flags(rs, (u32)OFF_F1Z + r * 128, 8, tag, lane);
      const int c7 = 7 * c;
#pragma unroll
      for (int m = 0; m < 2; ++m) {
        const u32 zo = (u32)OFF_Z1 + (8 * r + 2 * wid + m) * (28 * 1024) + lane * 16 + (c7 >> 1) * 1024;
        u32x4 zz[4];
#pragma unroll
        for (int k = 0; k < 4; ++k) zz[k] = ld16(rs, zo + k * 1024);
        const float bs = bf2f(bsp[128 * r + 32 * wid + 8 * (i16 >> 2) + 4 * m + (i16 & 3)]);
        u32x2 v[7];
#pragma unroll
        for (int j = 0; j < 7; ++j) {
          const u32x2 ze = ((j & 1) ? u32x2{zz[j >> 1].z, zz[j >> 1].w} : u32x2{zz[j >> 1].x, zz[j >> 1].y});
          const u32x2 zo_ = (((j + 1) & 1) ? u32x2{zz[(j + 1) >> 1].z, zz[(j + 1) >> 1].w} : u32x2{zz[(j + 1) >> 1].x, zz[(j + 1) >> 1].y});
          const u32x2 z = (c7 & 1) ? zo_ : ze;
          v[j] = u32x2{pk2(blo(z.x) * (acc[m][j][0] + bs), bhi(z.x) * (acc[m][j][1] + bs)), pk2(blo(z.y) * (acc[m][j][2] + bs), bhi(z.y) * (acc[m][j][3] + bs))};
        }
        store_pairs(rs, (u32)OFF_OMAT + (8 * r + 2 * wid + m) * (56 * 1024) + lane * 16, c7, v);
      }
    } else {
#pragma unroll
      for (int m = 0; m < 2; ++m) {
        u32x2 v[7];
#pragma unroll
        for (int j = 0; j < 7; ++j) v[j] = u32x2{pk2(acc[m][j][0], acc[m][j][1]), pk2(acc[m][j][2], acc[m][j][3])};
        store_pairs(rs, (u32)OFF_OMAT + (8 * r + 2 * wid + m) * (56 * 1024) + lane * 16, 7 * c, v);
      }
    }
    WAIT_V(0);
  }
  WAIT_L0();
  BAR();
  if (tid == 0) __hip_atomic_store((GAS u32*)(ws + OFF_F2 + r * 128 + c * 4), tag, RLX_AGENT);
  STAMP(9);
  if (!p3) return;

  {
    if (wid == 4) wait_flags(rs, (u32)OFF_F2 + (rb >> 1) * 128, 16, tag, lane);
    WAIT_V(0);
    BAR();
    STAMP(10);
    if (blockIdx.x == 0 && tid == 0) __hip_atomic_store((GAS u32*)(ws + OFF_EPOCH), tag, RLX_AGENT);
    const int tq = wid & 3, kh = wid >> 2;
    const int T = 4 * rb + tq;
    const u32 om = (u32)OFF_OMAT + T * (56 * 1024) + lane * 16;
    u32x4 hf[14];
#pragma unroll
    for (int p = 0; p < 14; ++p) hf[p] = ld16(rs, om + (14 * kh + p) * 1024);
    const u32x4 yr = ld16(rs, om + (28 + 2 * db + kh) * 1024);
    const u32x4 yi = ld16(rs, om + (42 + 2 * db + kh) * 1024);
    const int trow = 32 * (T >> 1) + 8 * (i16 >> 2) + 4 * (T & 1) + (i16 & 3);
    const float gate = __uint_as_float(ld4(rs, (u32)OFF_GATE + trow * 4));
    SCHED();
    f32x4 acc[4];
#pragma unroll
    for (int j = 0; j < 4; ++j) acc[j] = f32x4{0.f, 0.f, 0.f, 0.f};
    const int a = i16 >> 2, pp = lane & 3;
    const int rloc = 4 * q + a;
    const int sw = ((rloc >> 1) & 3) << 1;
    const char* tb0 = smem + (kh ? 4 : 2) * SB2 + rloc * 128 + (pp & 1) * 8;
    const char* tb1 = smem + (kh ? 0 : 3) * SB2 + rloc * 128 + (pp & 1) * 8;
    int co[4];
#pragma unroll
    for (int j = 0; j < 4; ++j) co[j] = ((2 * j + (pp >> 1)) ^ sw) << 4;
#pragma unroll
    for (int p = 0; p < 14; ++p) {
      const char* tb = (p < 7 ? tb0 : tb1) + (p % 7) * 4096;
#pragma unroll
      for (int j = 0; j < 4; ++j) {
        const bf16x8 wf = lds_tr2(tb + co[j], tb + co[j] + 2048);
        acc[j] = MFMA(wf, __builtin_bit_cast(bf16x8, hf[p]), acc[j]);
      }
    }
    STAMP(11);
    LAS f32x4* red = (LAS f32x4*)(LAS char*)(smem + 1 * SB2);
    red[(wid * 2 + 0) * 64 + lane] = kh ? acc[0] : acc[2];
    red[(wid * 2 + 1) * 64 + lane] = kh ? acc[1] : acc[3];
    WAIT_L0();
    BAR();
    const f32x4 o0 = (kh ? acc[2] : acc[0]) + red[((wid ^ 4) * 2 + 0) * 64 + lane];
    const f32x4 o1 = (kh ? acc[3] : acc[1]) + red[((wid ^ 4) * 2 + 1) * 64 + lane];
    const float og = 1.0f - gate;
    char* op = out + (size_t)trow * 896 + db * 128 + kh * 64 + q * 8;
    *(u32x2*)(op) = u32x2{pk2(gate * (blo(yr.x) - blo(yi.x)) + og * o0[0], gate * (bhi(yr.x) - bhi(yi.x)) + og * o0[1]),
                          pk2(gate * (blo(yr.y) - blo(yi.y)) + og * o0[2], gate * (bhi(yr.y) - bhi(yi.y)) + og * o0[3])};
    *(u32x2*)(op + 32) = u32x2{pk2(gate * (blo(yr.z) - blo(yi.z)) + og * o1[0], gate * (bhi(yr.z) - bhi(yi.z)) + og * o1[1]),
                               pk2(gate * (blo(yr.w) - blo(yi.w)) + og * o1[2], gate * (bhi(yr.w) - bhi(yi.w)) + og * o1[3])};
    STAMP(12);
  }
}

extern "C" void kernel_launch(void* const* d_in, const int* in_sizes, int n_in, void* d_out, int out_size, void* d_ws, size_t ws_size,
                              hipStream_t stream) {
  fused<<<256, 512, 0, stream>>>((const char*)d_in[0], (const char*)d_in[6], (const char*)d_in[3], (const char*)d_in[4], (const char*)d_in[10],
                                 (const char*)d_in[7], (const char*)d_in[1], (const char*)d_in[2], (const u16*)d_in[8], (const char*)d_in[9],
                                 (char*)d_ws, (char*)d_out);
}
```

```cpp
#include <hip/hip_runtime.h>
#include <hip/hip_bf16.h>

#define LAS __attribute__((address_space(3)))
#define GAS __attribute__((address_space(1)))
typedef unsigned short u16;
typedef unsigned int u32;
typedef unsigned long long u64;
typedef short bf16x8 __attribute__((ext_vector_type(8)));
typedef short bf16x4 __attribute__((ext_vector_type(4)));
typedef float f32x4 __attribute__((ext_vector_type(4)));
typedef float f32x2 __attribute__((ext_vector_type(2)));
typedef unsigned u32x4 __attribute__((ext_vector_type(4)));
typedef unsigned u32x2 __attribute__((ext_vector_type(2)));
typedef __bf16 bf2_t __attribute__((ext_vector_type(2)));

#define DI __device__ __forceinline__
#define MFMA(a, b, c) __builtin_amdgcn_mfma_f32_16x16x32_bf16((a), (b), (c), 0, 0, 0)
#define WAIT_V(n) asm volatile("s_waitcnt vmcnt(%0)" ::"i"(n) : "memory")
#define WAIT_L0() asm volatile("s_waitcnt lgkmcnt(0)" ::: "memory")
#define BAR() do { asm volatile("" ::: "memory"); __builtin_amdgcn_s_barrier(); asm volatile("" ::: "memory"); } while (0)
#define SCHED() __builtin_amdgcn_sched_barrier(0)
#define RLX_AGENT __ATOMIC_RELAXED, __HIP_MEMORY_SCOPE_AGENT

constexpr size_t BMAT_C = 458752;
constexpr size_t OFF_BMAT = 0;
constexpr size_t OFF_Z1 = OFF_BMAT + 16 * BMAT_C;
constexpr size_t OFF_OMAT = OFF_Z1 + 128 * 28 * 1024;
constexpr size_t OFF_GATE = OFF_OMAT + 128 * 56 * 1024;
constexpr size_t OFF_XCH = OFF_GATE + 2048 * 4;
constexpr u32 FS = 256;
constexpr size_t OFF_F1 = OFF_XCH + 16 * 14 * 128 * 16;
constexpr size_t OFF_F1Z = OFF_F1 + 256 * FS;
constexpr size_t OFF_F2 = OFF_F1Z + 256 * FS;
constexpr size_t OFF_FG = OFF_F2 + 256 * FS;
constexpr size_t OFF_EPOCH = OFF_FG + 32 * FS;
constexpr size_t WS_END = OFF_EPOCH + 128;
constexpr u32 WS_BYTES = (u32)WS_END;

DI u32 pk2(float lo, float hi) { bf2_t v = {(__bf16)lo, (__bf16)hi}; return __builtin_bit_cast(u32, v); }
DI float blo(u32 v) { return __uint_as_float(v << 16); }
DI float bhi(u32 v) { return __uint_as_float(v & 0xffff0000u); }
DI f32x2 gelu2(f32x2 x) {
  const f32x2 c2 = {-0.1029432397f, -0.1029432397f}, c1 = {-2.302208198f, -2.302208198f};
  f32x2 a = x * __builtin_elementwise_fma(x * x, c2, c1);
  asm("" : "+v"(a));
  f32x2 d = {__builtin_amdgcn_exp2f(a.x), __builtin_amdgcn_exp2f(a.y)};
  d = d + 1.0f;
  asm("" : "+v"(d));
  d = f32x2{__builtin_amdgcn_rcpf(d.x), __builtin_amdgcn_rcpf(d.y)};
  return x * d;
}
DI f32x4 gelu4(f32x4 x) {
  const f32x2 a = gelu2(__builtin_shufflevector(x, x, 0, 1)), b = gelu2(__builtin_shufflevector(x, x, 2, 3));
  return __builtin_shufflevector(a, b, 0, 1, 2, 3);
}
template <int CTRL> DI float dpp_add(float v) {
  float o = v + __builtin_bit_cast(float, __builtin_amdgcn_update_dpp(0, __builtin_bit_cast(int, v), CTRL, 0xf, 0xf, true));
  asm("" : "+v"(o));
  return o;
}
template <int CTRL> DI void dpp_add8(f32x4& a, f32x4& b) {
#pragma unroll
  for (int e = 0; e < 4; ++e) { a[e] = dpp_add<CTRL>(a[e]); b[e] = dpp_add<CTRL>(b[e]); }
}
DI void row16_sum8(f32x4& a, f32x4& b) { dpp_add8<0x128>(a, b); dpp_add8<0x124>(a, b); dpp_add8<0x122>(a, b); dpp_add8<0x121>(a, b); }
DI float half_sum(float v) {
  const auto p = __builtin_amdgcn_permlane32_swap(__float_as_uint(v), __float_as_uint(v), false, false);
  return __uint_as_float(p[0]) + __uint_as_float(p[1]);
}
DI bf16x8 lds_tr2(const char* p0, const char* p1) {
  bf16x4 a = __builtin_amdgcn_ds_read_tr16_b64_v4i16((LAS bf16x4*)(LAS char*)p0);
  bf16x4 b = __builtin_amdgcn_ds_read_tr16_b64_v4i16((LAS bf16x4*)(LAS char*)p1);
  return __builtin_shufflevector(a, b, 0, 1, 2, 3, 4, 5, 6, 7);
}
DI void glds(const char* base, u32 voff, u32 lds) {
  u64 t; asm volatile("s_mov_b32 m0, %3\n\ts_mov_b64 %0, %2\n\tglobal_load_lds_dwordx4 %1, %0" : "=&s"(t) : "v"(voff), "s"(base), "s"(lds) : "memory");
}
DI void touch(const char* base, u32 voff, u32 lds) {
  u64 t; asm volatile("s_mov_b32 m0, %3\n\ts_mov_b64 %0, %2\n\tglobal_load_lds_dword %1, %0" : "=&s"(t) : "v"(voff), "s"(base), "s"(lds) : "memory");
}
DI void glds4(const char* base, u32 v0, u32 v1, u32 v2, u32 v3, u32 lds) {
  u64 t; asm volatile("s_mov_b32 m0, %6\n\ts_mov_b64 %0, %5\n\tglobal_load_lds_dwordx4 %1, %0\n\tglobal_load_lds_dwordx4 %2, %0 offset:1024\n\t"
                      "global_load_lds_dwordx4 %3, %0 offset:2048\n\tglobal_load_lds_dwordx4 %4, %0 offset:3072"
                      : "=&s"(t) : "v"(v0), "v"(v1), "v"(v2), "v"(v3), "s"(base), "s"(lds) : "memory");
}
DI void glds2(const char* base, u32 v0, u32 v1, u32 lds) {
  u64 t; asm volatile("s_mov_b32 m0, %4\n\ts_mov_b64 %0, %3\n\tglobal_load_lds_dwordx4 %1, %0\n\tglobal_load_lds_dwordx4 %2, %0 offset:1024"
                      : "=&s"(t) : "v"(v0), "v"(v1), "s"(base), "s"(lds) : "memory");
}
template <int NB> DI void gldsB(const char* base, u32 v, u32 lds) {
  u64 t;
  if (NB == 4) asm volatile("s_mov_b32 m0, %3\n\ts_mov_b64 %0, %2\n\tglobal_load_lds_dwordx4 %1, %0 sc1\n\tglobal_load_lds_dwordx4 %1, %0 offset:1024 sc1\n\t"
                            "global_load_lds_dwordx4 %1, %0 offset:2048 sc1\n\tglobal_load_lds_dwordx4 %1, %0 offset:3072 sc1" : "=&s"(t) : "v"(v), "s"(base), "s"(lds) : "memory");
  else asm volatile("s_mov_b32 m0, %3\n\ts_mov_b64 %0, %2\n\tglobal_load_lds_dwordx4 %1, %0 sc1\n\tglobal_load_lds_dwordx4 %1, %0 offset:1024 sc1\n\t"
                    "global_load_lds_dwordx4 %1, %0 offset:2048 sc1" : "=&s"(t) : "v"(v), "s"(base), "s"(lds) : "memory");
}
template <int V> struct IC { static constexpr int value = V; };
DI void touch_ptr(const char* p, u32 lds) {
  asm volatile("s_mov_b32 m0, %1\n\ts_nop 0\n\tglobal_load_lds_dword %0, off sc1" :: "v"(p), "s"(lds) : "memory");
}
DI void flags_to_lds(const char* base, u32 voff, u32 lds) {
  u64 t; asm volatile("s_mov_b32 m0, %3\n\ts_mov_b64 %0, %2\n\tglobal_load_lds_dword %1, %0 sc1" : "=&s"(t) : "v"(voff), "s"(base), "s"(lds) : "memory");
}
#define PIN_S(v) asm volatile("" : "+s"(v))
typedef __amdgpu_buffer_rsrc_t rsrc_t;
DI void st16(rsrc_t rs, u32 off, u32x4 v) { __builtin_amdgcn_raw_buffer_store_b128(v, rs, off, 0, 16); }
DI void st8(rsrc_t rs, u32 off, u32x2 v) { __builtin_amdgcn_raw_buffer_store_b64(v, rs, off, 0, 16); }
DI u32x4 ld16(rsrc_t rs, u32 off) { return __builtin_amdgcn_raw_buffer_load_b128(rs, off, 0, 16); }
DI u32x4 ld8x2(rsrc_t rs, u32 o0, u32 o1) {
  const u32x2 a = __builtin_amdgcn_raw_buffer_load_b64(rs, o0, 0, 16), b = __builtin_amdgcn_raw_buffer_load_b64(rs, o1, 0, 16);
  return u32x4{a.x, a.y, b.x, b.y};
}
DI u32 ld4(rsrc_t rs, u32 off) { return __builtin_amdgcn_raw_buffer_load_b32(rs, off, 0, 16); }

DI void put_flag(rsrc_t rs, u32 off, u32 tag, int lane) { if (lane < 8) st16(rs, off + lane * 16, u32x4{tag, tag, tag, tag}); }
DI void wait_flags(rsrc_t rs, u32 off, int n, u32 tag, int lane) {
  const u64 t0 = __builtin_amdgcn_s_memrealtime();
  u32 o = lane < n ? off + lane * FS : 0xffffff00u;
  for (;;) {
    asm volatile("" ::: "memory");
    const u32 v = ld4(rs, o);
    if (v == tag) o = 0xffffff00u;
    if (__all(o == 0xffffff00u)) break;
    if (__builtin_amdgcn_s_memrealtime() - t0 > 2000000ull) break;
    __builtin_amdgcn_s_sleep(4);
  }
}

constexpr int SLOT = 32768;
constexpr int L_TAB = 4 * SLOT + 16384 + 14336;
constexpr int L_FIN = L_TAB, L_DUMMY = L_TAB + 1024, L_GO = L_TAB - 64, LDS_BYTES = 5 * SLOT;

DI int fresh_lane() { int l; asm volatile("v_mbcnt_lo_u32_b32 %0, -1, 0\n\tv_mbcnt_hi_u32_b32 %0, -1, %0" : "=v"(l)); return l; }
#define LANES() const int lane = fresh_lane(), q = lane >> 4, i16 = lane & 15; (void)q; (void)i16
#define A0() (lb + (32 * lw + i16) * 128 + ((q ^ ((i16 >> 1) & 7)) << 4))
constexpr int VMS = 3;
constexpr int HD = 6;
DI void loop_g(f32x4 (&acc)[2][7], int n, u32 a0, u32 b0) {
  asm volatile(
      "v_mov_b32 v157, %15\n\t"
      "v_xor_b32 v158, 64, %15\n\t"
      "v_mov_b32 v159, %16\n\t"
      "v_add_u32 v160, 0x10000, v157\n\t"
      "v_add_u32 v161, 0x10000, v158\n\t"
      "v_add_u32 v162, 0x10000, v159\n\t"
      "v_add_u32 v163, 0x20000, v157\n\t"
      "v_add_u32 v164, 0x20000, v158\n\t"
      "v_add_u32 v165, 0x20000, v159\n\t"
      "ds_read_b128 v[184:187], v157 offset:0\n\t"
      "ds_read_b128 v[188:191], v157 offset:2048\n\t"
      "ds_read_b128 v[192:195], v159 offset:0\n\t"
      "ds_read_b128 v[196:199], v159 offset:1024\n\t"
      "ds_read_b128 v[200:203], v159 offset:2048\n\t"
      "ds_read_b128 v[204:207], v159 offset:3072\n\t"
      "ds_read_b128 v[208:211], v159 offset:4096\n\t"
      "ds_read_b128 v[212:215], v159 offset:5120\n\t"
      "ds_read_b128 v[216:219], v159 offset:6144\n\t"
      ".p2align 3\n\t"
      "1:\n\t"
      "s_waitcnt lgkmcnt(5)\n\t"
      "v_mfma_f32_16x16x32_bf16 %0, v[192:195], v[184:187], %0\n\t"
      "ds_read_b128 v[220:223], v158 offset:0\n\t"
      "v_mfma_f32_16x16x32_bf16 %7, v[192:195], v[188:191], %7\n\t"
      "ds_read_b128 v[224:227], v158 offset:2048\n\t"
      "v_mfma_f32_16x16x32_bf16 %1, v[196:199], v[184:187], %1\n\t"
      "ds_read_b128 v[228:231], v159 offset:7168\n\t"
      "v_mfma_f32_16x16x32_bf16 %8, v[196:199], v[188:191], %8\n\t"
      "ds_read_b128 v[232:235], v159 offset:8192\n\t"
      "s_waitcnt lgkmcnt(7)\n\t"
      "v_mfma_f32_16x16x32_bf16 %2, v[200:203], v[184:187], %2\n\t"
      "ds_read_b128 v[236:239], v159 offset:9216\n\t"
      "v_mfma_f32_16x16x32_bf16 %9, v[200:203], v[188:191], %9\n\t"
      "ds_read_b128 v[240:243], v159 offset:10240\n\t"
      "v_mfma_f32_16x16x32_bf16 %3, v[204:207], v[184:187], %3\n\t"
      "ds_read_b128 v[244:247], v159 offset:11264\n\t"
      "v_mfma_f32_16x16x32_bf16 %10, v[204:207], v[188:191], %10\n\t"
      "ds_read_b128 v[248:251], v159 offset:12288\n\t"
      "s_waitcnt lgkmcnt(9)\n\t"
      "v_mfma_f32_16x16x32_bf16 %4, v[208:211], v[184:187], %4\n\t"
      "ds_read_b128 v[252:255], v159 offset:13312\n\t"
      "v_mfma_f32_16x16x32_bf16 %11, v[208:211], v[188:191], %11\n\t"
      "v_mfma_f32_16x16x32_bf16 %5, v[212:215], v[184:187], %5\n\t"
      "v_mfma_f32_16x16x32_bf16 %12, v[212:215], v[188:191], %12\n\t"
      "s_waitcnt lgkmcnt(9)\n\t"
      "v_mfma_f32_16x16x32_bf16 %6, v[216:219], v[184:187], %6\n\t"
      "s_sub_u32 %14, %14, 1\n\t"
      "v_mfma_f32_16x16x32_bf16 %13, v[216:219], v[188:191], %13\n\t"
      "s_waitcnt lgkmcnt(5)\n\t"
      "v_mfma_f32_16x16x32_bf16 %0, v[228:231], v[220:223], %0\n\t"
      "v_mfma_f32_16x16x32_bf16 %7, v[228:231], v[224:227], %7\n\t"
      "s_waitcnt lgkmcnt(0)\n\t"
      "s_barrier\n\t"
      "v_mfma_f32_16x16x32_bf16 %1, v[232:235], v[220:223], %1\n\t"
      "ds_read_b128 v[184:187], v157 offset:32768\n\t"
      "v_mfma_f32_16x16x32_bf16 %8, v[232:235], v[224:227], %8\n\t"
      "ds_read_b128 v[188:191], v157 offset:34816\n\t"
      "v_mfma_f32_16x16x32_bf16 %2, v[236:239], v[220:223], %2\n\t"
      "ds_read_b128 v[192:195], v159 offset:32768\n\t"
      "v_mfma_f32_16x16x32_bf16 %9, v[236:239], v[224:227], %9\n\t"
      "ds_read_b128 v[196:199], v159 offset:33792\n\t"
      "v_mfma_f32_16x16x32_bf16 %3, v[240:243], v[220:223], %3\n\t"
      "ds_read_b128 v[200:203], v159 offset:34816\n\t"
      "v_mfma_f32_16x16x32_bf16 %10, v[240:243], v[224:227], %10\n\t"
      "ds_read_b128 v[204:207], v159 offset:35840\n\t"
      "v_mfma_f32_16x16x32_bf16 %4, v[244:247], v[220:223], %4\n\t"
      "ds_read_b128 v[208:211], v159 offset:36864\n\t"
      "v_mfma_f32_16x16x32_bf16 %11, v[244:247], v[224:227], %11\n\t"
      "ds_read_b128 v[212:215], v159 offset:37888\n\t"
      "v_mfma_f32_16x16x32_bf16 %5, v[248:251], v[220:223], %5\n\t"
      "ds_read_b128 v[216:219], v159 offset:38912\n\t"
      "v_mfma_f32_16x16x32_bf16 %12, v[248:251], v[224:227], %12\n\t"
      "v_mfma_f32_16x16x32_bf16 %6, v[252:255], v[220:223], %6\n\t"
      "s_cmp_eq_u32 %14, 0\n\t"
      "v_mfma_f32_16x16x32_bf16 %13, v[252:255], v[224:227], %13\n\t"
      "s_cbranch_scc1 2f\n\t"
      "s_waitcnt lgkmcnt(5)\n\t"
      "v_mfma_f32_16x16x32_bf16 %0, v[192:195], v[184:187], %0\n\t"
      "ds_read_b128 v[220:223], v158 offset:32768\n\t"
      "v_mfma_f32_16x16x32_bf16 %7, v[192:195], v[188:191], %7\n\t"
      "ds_read_b128 v[224:227], v158 offset:34816\n\t"
      "v_mfma_f32_16x16x32_bf16 %1, v[196:199], v[184:187], %1\n\t"
      "ds_read_b128 v[228:231], v159 offset:39936\n\t"
      "v_mfma_f32_16x16x32_bf16 %8, v[196:199], v[188:191], %8\n\t"
      "ds_read_b128 v[232:235], v159 offset:40960\n\t"
      "s_waitcnt lgkmcnt(7)\n\t"
      "v_mfma_f32_16x16x32_bf16 %2, v[200:203], v[184:187], %2\n\t"
      "ds_read_b128 v[236:239], v159 offset:41984\n\t"
      "v_mfma_f32_16x16x32_bf16 %9, v[200:203], v[188:191], %9\n\t"
      "ds_read_b128 v[240:243], v159 offset:43008\n\t"
      "v_mfma_f32_16x16x32_bf16 %3, v[204:207], v[184:187], %3\n\t"
      "ds_read_b128 v[244:247], v159 offset:44032\n\t"
      "v_mfma_f32_16x16x32_bf16 %10, v[204:207], v[188:191], %10\n\t"
      "ds_read_b128 v[248:251], v159 offset:45056\n\t"
      "s_waitcnt lgkmcnt(9)\n\t"
      "v_mfma_f32_16x16x32_bf16 %4, v[208:211], v[184:187], %4\n\t"
      "ds_read_b128 v[252:255], v159 offset:46080\n\t"
      "v_mfma_f32_16x16x32_bf16 %11, v[208:211], v[188:191], %11\n\t"
      "v_mfma_f32_16x16x32_bf16 %5, v[212:215], v[184:187], %5\n\t"
      "v_mfma_f32_16x16x32_bf16 %12, v[212:215], v[188:191], %12\n\t"
      "s_waitcnt lgkmcnt(9)\n\t"
      "v_mfma_f32_16x16x32_bf16 %6, v[216:219], v[184:187], %6\n\t"
      "s_sub_u32 %14, %14, 1\n\t"
      "v_mfma_f32_16x16x32_bf16 %13, v[216:219], v[188:191], %13\n\t"
      "s_waitcnt lgkmcnt(5)\n\t"
      "v_mfma_f32_16x16x32_bf16 %0, v[228:231], v[220:223], %0\n\t"
      "v_mfma_f32_16x16x32_bf16 %7, v[228:231], v[224:227], %7\n\t"
      "s_waitcnt lgkmcnt(0)\n\t"
      "s_barrier\n\t"
      "v_mfma_f32_16x16x32_bf16 %1, v[232:235], v[220:223], %1\n\t"
      "ds_read_b128 v[184:187], v160 offset:0\n\t"
      "v_mfma_f32_16x16x32_bf16 %8, v[232:235], v[224:227], %8\n\t"
      "ds_read_b128 v[188:191], v160 offset:2048\n\t"
      "v_mfma_f32_16x16x32_bf16 %2, v[236:239], v[220:223], %2\n\t"
      "ds_read_b128 v[192:195], v162 offset:0\n\t"
      "v_mfma_f32_16x16x32_bf16 %9, v[236:239], v[224:227], %9\n\t"
      "ds_read_b128 v[196:199], v162 offset:1024\n\t"
      "v_mfma_f32_16x16x32_bf16 %3, v[240:243], v[220:223], %3\n\t"
      "ds_read_b128 v[200:203], v162 offset:2048\n\t"
      "v_mfma_f32_16x16x32_bf16 %10, v[240:243], v[224:227], %10\n\t"
      "ds_read_b128 v[204:207], v162 offset:3072\n\t"
      "v_mfma_f32_16x16x32_bf16 %4, v[244:247], v[220:223], %4\n\t"
      "ds_read_b128 v[208:211], v162 offset:4096\n\t"
      "v_mfma_f32_16x16x32_bf16 %11, v[244:247], v[224:227], %11\n\t"
      "ds_read_b128 v[212:215], v162 offset:5120\n\t"
      "v_mfma_f32_16x16x32_bf16 %5, v[248:251], v[220:223], %5\n\t"
      "ds_read_b128 v[216:219], v162 offset:6144\n\t"
      "v_mfma_f32_16x16x32_bf16 %12, v[248:251], v[224:227], %12\n\t"
      "v_mfma_f32_16x16x32_bf16 %6, v[252:255], v[220:223], %6\n\t"
      "s_cmp_eq_u32 %14, 0\n\t"
      "v_mfma_f32_16x16x32_bf16 %13, v[252:255], v[224:227], %13\n\t"
      "s_cbranch_scc1 2f\n\t"
      "s_waitcnt lgkmcnt(5)\n\t"
      "v_mfma_f32_16x16x32_bf16 %0, v[192:195], v[184:187], %0\n\t"
      "ds_read_b128 v[220:223], v161 offset:0\n\t"
      "v_mfma_f32_16x16x32_bf16 %7, v[192:195], v[188:191], %7\n\t"
      "ds_read_b128 v[224:227], v161 offset:2048\n\t"
      "v_mfma_f32_16x16x32_bf16 %1, v[196:199], v[184:187], %1\n\t"
      "ds_read_b128 v[228:231], v162 offset:7168\n\t"
      "v_mfma_f32_16x16x32_bf16 %8, v[196:199], v[188:191], %8\n\t"
      "ds_read_b128 v[232:235], v162 offset:8192\n\t"
      "s_waitcnt lgkmcnt(7)\n\t"
      "v_mfma_f32_16x16x32_bf16 %2, v[200:203], v[184:187], %2\n\t"
      "ds_read_b128 v[236:239], v162 offset:9216\n\t"
      "v_mfma_f32_16x16x32_bf16 %9, v[200:203], v[188:191], %9\n\t"
      "ds_read_b128 v[240:243], v162 offset:10240\n\t"
      "v_mfma_f32_16x16x32_bf16 %3, v[204:207], v[184:187], %3\n\t"
      "ds_read_b128 v[244:247], v162 offset:11264\n\t"
      "v_mfma_f32_16x16x32_bf16 %10, v[204:207], v[188:191], %10\n\t"
      "ds_read_b128 v[248:251], v162 offset:12288\n\t"
      "s_waitcnt lgkmcnt(9)\n\t"
      "v_mfma_f32_16x16x32_bf16 %4, v[208:211], v[184:187], %4\n\t"
      "ds_read_b128 v[252:255], v162 offset:13312\n\t"
      "v_mfma_f32_16x16x32_bf16 %11, v[208:211], v[188:191], %11\n\t"
      "v_mfma_f32_16x16x32_bf16 %5, v[212:215], v[184:187], %5\n\t"
      "v_mfma_f32_16x16x32_bf16 %12, v[212:215], v[188:191], %12\n\t"
      "s_waitcnt lgkmcnt(9)\n\t"
      "v_mfma_f32_16x16x32_bf16 %6, v[216:219], v[184:187], %6\n\t"
      "s_sub_u32 %14, %14, 1\n\t"
      "v_mfma_f32_16x16x32_bf16 %13, v[216:219], v[188:191], %13\n\t"
      "s_waitcnt lgkmcnt(5)\n\t"
      "v_mfma_f32_16x16x32_bf16 %0, v[228:231], v[220:223], %0\n\t"
      "v_mfma_f32_16x16x32_bf16 %7, v[228:231], v[224:227], %7\n\t"
      "s_waitcnt lgkmcnt(0)\n\t"
      "s_barrier\n\t"
      "v_mfma_f32_16x16x32_bf16 %1, v[232:235], v[220:223], %1\n\t"
      "ds_read_b128 v[184:187], v160 offset:32768\n\t"
      "v_mfma_f32_16x16x32_bf16 %8, v[232:235], v[224:227], %8\n\t"
      "ds_read_b128 v[188:191], v160 offset:34816\n\t"
      "v_mfma_f32_16x16x32_bf16 %2, v[236:239], v[220:223], %2\n\t"
      "ds_read_b128 v[192:195], v162 offset:32768\n\t"
      "v_mfma_f32_16x16x32_bf16 %9, v[236:239], v[224:227], %9\n\t"
      "ds_read_b128 v[196:199], v162 offset:33792\n\t"
      "v_mfma_f32_16x16x32_bf16 %3, v[240:243], v[220:223], %3\n\t"
      "ds_read_b128 v[200:203], v162 offset:34816\n\t"
      "v_mfma_f32_16x16x32_bf16 %10, v[240:243], v[224:227], %10\n\t"
      "ds_read_b128 v[204:207], v162 offset:35840\n\t"
      "v_mfma_f32_16x16x32_bf16 %4, v[244:247], v[220:223], %4\n\t"
      "ds_read_b128 v[208:211], v162 offset:36864\n\t"
      "v_mfma_f32_16x16x32_bf16 %11, v[244:247], v[224:227], %11\n\t"
      "ds_read_b128 v[212:215], v162 offset:37888\n\t"
      "v_mfma_f32_16x16x32_bf16 %5, v[248:251], v[220:223], %5\n\t"
      "ds_read_b128 v[216:219], v162 offset:38912\n\t"
      "v_mfma_f32_16x16x32_bf16 %12, v[248:251], v[224:227], %12\n\t"
      "v_mfma_f32_16x16x32_bf16 %6, v[252:255], v[220:223], %6\n\t"
      "s_cmp_eq_u32 %14, 0\n\t"
      "v_mfma_f32_16x16x32_bf16 %13, v[252:255], v[224:227], %13\n\t"
      "s_cbranch_scc1 2f\n\t"
      "s_waitcnt lgkmcnt(5)\n\t"
      "v_mfma_f32_16x16x32_bf16 %0, v[192:195], v[184:187], %0\n\t"
      "ds_read_b128 v[220:223], v161 offset:32768\n\t"
      "v_mfma_f32_16x16x32_bf16 %7, v[192:195], v[188:191], %7\n\t"
      "ds_read_b128 v[224:227], v161 offset:34816\n\t"
      "v_mfma_f32_16x16x32_bf16 %1, v[196:199], v[184:187], %1\n\t"
      "ds_read_b128 v[228:231], v162 offset:39936\n\t"
      "v_mfma_f32_16x16x32_bf16 %8, v[196:199], v[188:191], %8\n\t"
      "ds_read_b128 v[232:235], v162 offset:40960\n\t"
      "s_waitcnt lgkmcnt(7)\n\t"
      "v_mfma_f32_16x16x32_bf16 %2, v[200:203], v[184:187], %2\n\t"
      "ds_read_b128 v[236:239], v162 offset:41984\n\t"
      "v_mfma_f32_16x16x32_bf16 %9, v[200:203], v[188:191], %9\n\t"
      "ds_read_b128 v[240:243], v162 offset:43008\n\t"
      "v_mfma_f32_16x16x32_bf16 %3, v[204:207], v[184:187], %3\n\t"
      "ds_read_b128 v[244:247], v162 offset:44032\n\t"
      "v_mfma_f32_16x16x32_bf16 %10, v[204:207], v[188:191], %10\n\t"
      "ds_read_b128 v[248:251], v162 offset:45056\n\t"
      "s_waitcnt lgkmcnt(9)\n\t"
      "v_mfma_f32_16x16x32_bf16 %4, v[208:211], v[184:187], %4\n\t"
      "ds_read_b128 v[252:255], v162 offset:46080\n\t"
      "v_mfma_f32_16x16x32_bf16 %11, v[208:211], v[188:191], %11\n\t"
      "v_mfma_f32_16x16x32_bf16 %5, v[212:215], v[184:187], %5\n\t"
      "v_mfma_f32_16x16x32_bf16 %12, v[212:215], v[188:191], %12\n\t"
      "s_waitcnt lgkmcnt(9)\n\t"
      "v_mfma_f32_16x16x32_bf16 %6, v[216:219], v[184:187], %6\n\t"
      "s_sub_u32 %14, %14, 1\n\t"
      "v_mfma_f32_16x16x32_bf16 %13, v[216:219], v[188:191], %13\n\t"
      "s_waitcnt lgkmcnt(5)\n\t"
      "v_mfma_f32_16x16x32_bf16 %0, v[228:231], v[220:223], %0\n\t"
      "v_mfma_f32_16x16x32_bf16 %7, v[228:231], v[224:227], %7\n\t"
      "s_waitcnt lgkmcnt(0)\n\t"
      "s_waitcnt vmcnt(0)\n\t"
      "s_barrier\n\t"
      "v_mfma_f32_16x16x32_bf16 %1, v[232:235], v[220:223], %1\n\t"
      "ds_read_b128 v[184:187], v163 offset:0\n\t"
      "v_mfma_f32_16x16x32_bf16 %8, v[232:235], v[224:227], %8\n\t"
      "ds_read_b128 v[188:191], v163 offset:2048\n\t"
      "v_mfma_f32_16x16x32_bf16 %2, v[236:239], v[220:223], %2\n\t"
      "ds_read_b128 v[192:195], v165 offset:0\n\t"
      "v_mfma_f32_16x16x32_bf16 %9, v[236:239], v[224:227], %9\n\t"
      "ds_read_b128 v[196:199], v165 offset:1024\n\t"
      "v_mfma_f32_16x16x32_bf16 %3, v[240:243], v[220:223], %3\n\t"
      "ds_read_b128 v[200:203], v165 offset:2048\n\t"
      "v_mfma_f32_16x16x32_bf16 %10, v[240:243], v[224:227], %10\n\t"
      "ds_read_b128 v[204:207], v165 offset:3072\n\t"
      "v_mfma_f32_16x16x32_bf16 %4, v[244:247], v[220:223], %4\n\t"
      "ds_read_b128 v[208:211], v165 offset:4096\n\t"
      "v_mfma_f32_16x16x32_bf16 %11, v[244:247], v[224:227], %11\n\t"
      "ds_read_b128 v[212:215], v165 offset:5120\n\t"
      "v_mfma_f32_16x16x32_bf16 %5, v[248:251], v[220:223], %5\n\t"
      "ds_read_b128 v[216:219], v165 offset:6144\n\t"
      "v_mfma_f32_16x16x32_bf16 %12, v[248:251], v[224:227], %12\n\t"
      "v_mfma_f32_16x16x32_bf16 %6, v[252:255], v[220:223], %6\n\t"
      "s_cmp_eq_u32 %14, 0\n\t"
      "v_mfma_f32_16x16x32_bf16 %13, v[252:255], v[224:227], %13\n\t"
      "s_cbranch_scc1 2f\n\t"
      "s_waitcnt lgkmcnt(5)\n\t"
      "v_mfma_f32_16x16x32_bf16 %0, v[192:195], v[184:187], %0\n\t"
      "ds_read_b128 v[220:223], v164 offset:0\n\t"
      "v_mfma_f32_16x16x32_bf16 %7, v[192:195], v[188:191], %7\n\t"
      "ds_read_b128 v[224:227], v164 offset:2048\n\t"
      "v_mfma_f32_16x16x32_bf16 %1, v[196:199], v[184:187], %1\n\t"
      "ds_read_b128 v[228:231], v165 offset:7168\n\t"
      "v_mfma_f32_16x16x32_bf16 %8, v[196:199], v[188:191], %8\n\t"
      "ds_read_b128 v[232:235], v165 offset:8192\n\t"
      "s_waitcnt lgkmcnt(7)\n\t"
      "v_mfma_f32_16x16x32_bf16 %2, v[200:203], v[184:187], %2\n\t"
      "ds_read_b128 v[236:239], v165 offset:9216\n\t"
      "v_mfma_f32_16x16x32_bf16 %9, v[200:203], v[188:191], %9\n\t"
      "ds_read_b128 v[240:243], v165 offset:10240\n\t"
      "v_mfma_f32_16x16x32_bf16 %3, v[204:207], v[184:187], %3\n\t"
      "ds_read_b128 v[244:247], v165 offset:11264\n\t"
      "v_mfma_f32_16x16x32_bf16 %10, v[204:207], v[188:191], %10\n\t"
      "ds_read_b128 v[248:251], v165 offset:12288\n\t"
      "s_waitcnt lgkmcnt(9)\n\t"
      "v_mfma_f32_16x16x32_bf16 %4, v[208:211], v[184:187], %4\n\t"
      "ds_read_b128 v[252:255], v165 offset:13312\n\t"
      "v_mfma_f32_16x16x32_bf16 %11, v[208:211], v[188:191], %11\n\t"
      "v_mfma_f32_16x16x32_bf16 %5, v[212:215], v[184:187], %5\n\t"
      "v_mfma_f32_16x16x32_bf16 %12, v[212:215], v[188:191], %12\n\t"
      "s_waitcnt lgkmcnt(9)\n\t"
      "v_mfma_f32_16x16x32_bf16 %6, v[216:219], v[184:187], %6\n\t"
      "s_sub_u32 %14, %14, 1\n\t"
      "v_mfma_f32_16x16x32_bf16 %13, v[216:219], v[188:191], %13\n\t"
      "s_waitcnt lgkmcnt(5)\n\t"
      "v_mfma_f32_16x16x32_bf16 %0, v[228:231], v[220:223], %0\n\t"
      "v_mfma_f32_16x16x32_bf16 %7, v[228:231], v[224:227], %7\n\t"
      "s_waitcnt lgkmcnt(0)\n\t"
      "s_barrier\n\t"
      "v_mfma_f32_16x16x32_bf16 %1, v[232:235], v[220:223], %1\n\t"
      "ds_read_b128 v[184:187], v157 offset:0\n\t"
      "v_mfma_f32_16x16x32_bf16 %8, v[232:235], v[224:227], %8\n\t"
      "ds_read_b128 v[188:191], v157 offset:2048\n\t"
      "v_mfma_f32_16x16x32_bf16 %2, v[236:239], v[220:223], %2\n\t"
      "ds_read_b128 v[192:195], v159 offset:0\n\t"
      "v_mfma_f32_16x16x32_bf16 %9, v[236:239], v[224:227], %9\n\t"
      "ds_read_b128 v[196:199], v159 offset:1024\n\t"
      "v_mfma_f32_16x16x32_bf16 %3, v[240:243], v[220:223], %3\n\t"
      "ds_read_b128 v[200:203], v159 offset:2048\n\t"
      "v_mfma_f32_16x16x32_bf16 %10, v[240:243], v[224:227], %10\n\t"
      "ds_read_b128 v[204:207], v159 offset:3072\n\t"
      "v_mfma_f32_16x16x32_bf16 %4, v[244:247], v[220:223], %4\n\t"
      "ds_read_b128 v[208:211], v159 offset:4096\n\t"
      "v_mfma_f32_16x16x32_bf16 %11, v[244:247], v[224:227], %11\n\t"
      "ds_read_b128 v[212:215], v159 offset:5120\n\t"
      "v_mfma_f32_16x16x32_bf16 %5, v[248:251], v[220:223], %5\n\t"
      "ds_read_b128 v[216:219], v159 offset:6144\n\t"
      "v_mfma_f32_16x16x32_bf16 %12, v[248:251], v[224:227], %12\n\t"
      "v_mfma_f32_16x16x32_bf16 %6, v[252:255], v[220:223], %6\n\t"
      "s_cmp_eq_u32 %14, 0\n\t"
      "v_mfma_f32_16x16x32_bf16 %13, v[252:255], v[224:227], %13\n\t"
      "s_cbranch_scc0 1b\n\t"
      "2:\n\t"
      "s_waitcnt lgkmcnt(0)\n\t"
      "s_nop 7\n\t"
      "s_nop 1\n\t"
      : "+v"(acc[0][0]), "+v"(acc[0][1]), "+v"(acc[0][2]), "+v"(acc[0][3]), "+v"(acc[0][4]), "+v"(acc[0][5]), "+v"(acc[0][6]), "+v"(acc[1][0]), "+v"(acc[1][1]), "+v"(acc[1][2]), "+v"(acc[1][3]), "+v"(acc[1][4]), "+v"(acc[1][5]), "+v"(acc[1][6]), "+s"(n)
      : "v"(a0), "v"(b0)
      : "memory", "scc", "v157", "v158", "v159", "v160", "v161", "v162", "v163", "v164", "v165", "v166", "v184", "v185", "v186", "v187", "v188", "v189", "v190", "v191", "v192", "v193", "v194", "v195", "v196", "v197", "v198", "v199", "v200", "v201", "v202", "v203", "v204", "v205", "v206", "v207", "v208", "v209", "v210", "v211", "v212", "v213", "v214", "v215", "v216", "v217", "v218", "v219", "v220", "v221", "v222", "v223", "v224", "v225", "v226", "v227", "v228", "v229", "v230", "v231", "v232", "v233", "v234", "v235", "v236", "v237", "v238", "v239", "v240", "v241", "v242", "v243", "v244", "v245", "v246", "v247", "v248", "v249", "v250", "v251", "v252", "v253", "v254", "v255");
}
DI void loop_gz(f32x4 (&acc)[2][7], u32x4 (&zz)[2][4], int n, u32 a0, u32 b0, u32 zvoff, const char* zbase) {
  asm volatile(
      "v_mov_b32 v157, %23\n\t"
      "v_xor_b32 v158, 64, %23\n\t"
      "v_mov_b32 v159, %24\n\t"
      "v_add_u32 v160, 0x10000, v157\n\t"
      "v_add_u32 v161, 0x10000, v158\n\t"
      "v_add_u32 v162, 0x10000, v159\n\t"
      "v_add_u32 v163, 0x20000, v157\n\t"
      "v_add_u32 v164, 0x20000, v158\n\t"
      "v_add_u32 v165, 0x20000, v159\n\t"
      "ds_read_b128 v[184:187], v157 offset:0\n\t"
      "ds_read_b128 v[188:191], v157 offset:2048\n\t"
      "ds_read_b128 v[192:195], v159 offset:0\n\t"
      "ds_read_b128 v[196:199], v159 offset:1024\n\t"
      "ds_read_b128 v[200:203], v159 offset:2048\n\t"
      "ds_read_b128 v[204:207], v159 offset:3072\n\t"
      "ds_read_b128 v[208:211], v159 offset:4096\n\t"
      "ds_read_b128 v[212:215], v159 offset:5120\n\t"
      "ds_read_b128 v[216:219], v159 offset:6144\n\t"
      "v_add_u32 v166, 0x7000, %25\n\t"
      "s_mov_b64 s[90:91], %26\n\t"
      ".p2align 3\n\t"
      "1:\n\t"
      "s_cmp_eq_u32 %14, 12\n\t"
      "s_cbranch_scc0 3f\n\t"
      "global_load_dwordx4 %15, %25, s[90:91] offset:0 sc1\n\t"
      "global_load_dwordx4 %16, %25, s[90:91] offset:1024 sc1\n\t"
      "global_load_dwordx4 %17, %25, s[90:91] offset:2048 sc1\n\t"
      "global_load_dwordx4 %18, %25, s[90:91] offset:3072 sc1\n\t"
      "global_load_dwordx4 %19, v166, s[90:91] offset:0 sc1\n\t"
      "global_load_dwordx4 %20, v166, s[90:91] offset:1024 sc1\n\t"
      "global_load_dwordx4 %21, v166, s[90:91] offset:2048 sc1\n\t"
      "global_load_dwordx4 %22, v166, s[90:91] offset:3072 sc1\n\t"
      "3:\n\t"
      "s_waitcnt lgkmcnt(5)\n\t"
      "v_mfma_f32_16x16x32_bf16 %0, v[192:195], v[184:187], %0\n\t"
      "ds_read_b128 v[220:223], v158 offset:0\n\t"
      "v_mfma_f32_16x16x32_bf16 %7, v[192:195], v[188:191], %7\n\t"
      "ds_read_b128 v[224:227], v158 offset:2048\n\t"
      "v_mfma_f32_16x16x32_bf16 %1, v[196:199], v[184:187], %1\n\t"
      "ds_read_b128 v[228:231], v159 offset:7168\n\t"
      "v_mfma_f32_16x16x32_bf16 %8, v[196:199], v[188:191], %8\n\t"
      "ds_read_b128 v[232:235], v159 offset:8192\n\t"
      "s_waitcnt lgkmcnt(7)\n\t"
      "v_mfma_f32_16x16x32_bf16 %2, v[200:203], v[184:187], %2\n\t"
      "ds_read_b128 v[236:239], v159 offset:9216\n\t"
      "v_mfma_f32_16x16x32_bf16 %9, v[200:203], v[188:191], %9\n\t"
      "ds_read_b128 v[240:243], v159 offset:10240\n\t"
      "v_mfma_f32_16x16x32_bf16 %3, v[204:207], v[184:187], %3\n\t"
      "ds_read_b128 v[244:247], v159 offset:11264\n\t"
      "v_mfma_f32_16x16x32_bf16 %10, v[204:207], v[188:191], %10\n\t"
      "ds_read_b128 v[248:251], v159 offset:12288\n\t"
      "s_waitcnt lgkmcnt(9)\n\t"
      "v_mfma_f32_16x16x32_bf16 %4, v[208:211], v[184:187], %4\n\t"
      "ds_read_b128 v[252:255], v159 offset:13312\n\t"
      "v_mfma_f32_16x16x32_bf16 %11, v[208:211], v[188:191], %11\n\t"
      "v_mfma_f32_16x16x32_bf16 %5, v[212:215], v[184:187], %5\n\t"
      "v_mfma_f32_16x16x32_bf16 %12, v[212:215], v[188:191], %12\n\t"
      "s_waitcnt lgkmcnt(9)\n\t"
      "v_mfma_f32_16x16x32_bf16 %6, v[216:219], v[184:187], %6\n\t"
      "s_sub_u32 %14, %14, 1\n\t"
      "v_mfma_f32_16x16x32_bf16 %13, v[216:219], v[188:191], %13\n\t"
      "s_waitcnt lgkmcnt(5)\n\t"
      "v_mfma_f32_16x16x32_bf16 %0, v[228:231], v[220:223], %0\n\t"
      "v_mfma_f32_16x16x32_bf16 %7, v[228:231], v[224:227], %7\n\t"
      "s_waitcnt lgkmcnt(0)\n\t"
      "s_barrier\n\t"
      "v_mfma_f32_16x16x32_bf16 %1, v[232:235], v[220:223], %1\n\t"
      "ds_read_b128 v[184:187], v157 offset:32768\n\t"
      "v_mfma_f32_16x16x32_bf16 %8, v[232:235], v[224:227], %8\n\t"
      "ds_read_b128 v[188:191], v157 offset:34816\n\t"
      "v_mfma_f32_16x16x32_bf16 %2, v[236:239], v[220:223], %2\n\t"
      "ds_read_b128 v[192:195], v159 offset:32768\n\t"
      "v_mfma_f32_16x16x32_bf16 %9, v[236:239], v[224:227], %9\n\t"
      "ds_read_b128 v[196:199], v159 offset:33792\n\t"
      "v_mfma_f32_16x16x32_bf16 %3, v[240:243], v[220:223], %3\n\t"
      "ds_read_b128 v[200:203], v159 offset:34816\n\t"
      "v_mfma_f32_16x16x32_bf16 %10, v[240:243], v[224:227], %10\n\t"
      "ds_read_b128 v[204:207], v159 offset:35840\n\t"
      "v_mfma_f32_16x16x32_bf16 %4, v[244:247], v[220:223], %4\n\t"
      "ds_read_b128 v[208:211], v159 offset:36864\n\t"
      "v_mfma_f32_16x16x32_bf16 %11, v[244:247], v[224:227], %11\n\t"
      "ds_read_b128 v[212:215], v159 offset:37888\n\t"
      "v_mfma_f32_16x16x32_bf16 %5, v[248:251], v[220:223], %5\n\t"
      "ds_read_b128 v[216:219], v159 offset:38912\n\t"
      "v_mfma_f32_16x16x32_bf16 %12, v[248:251], v[224:227], %12\n\t"
      "v_mfma_f32_16x16x32_bf16 %6, v[252:255], v[220:223], %6\n\t"
      "s_cmp_eq_u32 %14, 0\n\t"
      "v_mfma_f32_16x16x32_bf16 %13, v[252:255], v[224:227], %13\n\t"
      "s_cbranch_scc1 2f\n\t"
      "s_waitcnt lgkmcnt(5)\n\t"
      "v_mfma_f32_16x16x32_bf16 %0, v[192:195], v[184:187], %0\n\t"
      "ds_read_b128 v[220:223], v158 offset:32768\n\t"
      "v_mfma_f32_16x16x32_bf16 %7, v[192:195], v[188:191], %7\n\t"
      "ds_read_b128 v[224:227], v158 offset:34816\n\t"
      "v_mfma_f32_16x16x32_bf16 %1, v[196:199], v[184:187], %1\n\t"
      "ds_read_b128 v[228:231], v159 offset:39936\n\t"
      "v_mfma_f32_16x16x32_bf16 %8, v[196:199], v[188:191], %8\n\t"
      "ds_read_b128 v[232:235], v159 offset:40960\n\t"
      "s_waitcnt lgkmcnt(7)\n\t"
      "v_mfma_f32_16x16x32_bf16 %2, v[200:203], v[184:187], %2\n\t"
      "ds_read_b128 v[236:239], v159 offset:41984\n\t"
      "v_mfma_f32_16x16x32_bf16 %9, v[200:203], v[188:191], %9\n\t"
      "ds_read_b128 v[240:243], v159 offset:43008\n\t"
      "v_mfma_f32_16x16x32_bf16 %3, v[204:207], v[184:187], %3\n\t"
      "ds_read_b128 v[244:247], v159 offset:44032\n\t"
      "v_mfma_f32_16x16x32_bf16 %10, v[204:207], v[188:191], %10\n\t"
      "ds_read_b128 v[248:251], v159 offset:45056\n\t"
      "s_waitcnt lgkmcnt(9)\n\t"
      "v_mfma_f32_16x16x32_bf16 %4, v[208:211], v[184:187], %4\n\t"
      "ds_read_b128 v[252:255], v159 offset:46080\n\t"
      "v_mfma_f32_16x16x32_bf16 %11, v[208:211], v[188:191], %11\n\t"
      "v_mfma_f32_16x16x32_bf16 %5, v[212:215], v[184:187], %5\n\t"
      "v_mfma_f32_16x16x32_bf16 %12, v[212:215], v[188:191], %12\n\t"
      "s_waitcnt lgkmcnt(9)\n\t"
      "v_mfma_f32_16x16x32_bf16 %6, v[216:219], v[184:187], %6\n\t"
      "s_sub_u32 %14, %14, 1\n\t"
      "v_mfma_f32_16x16x32_bf16 %13, v[216:219], v[188:191], %13\n\t"
      "s_waitcnt lgkmcnt(5)\n\t"
      "v_mfma_f32_16x16x32_bf16 %0, v[228:231], v[220:223], %0\n\t"
      "v_mfma_f32_16x16x32_bf16 %7, v[228:231], v[224:227], %7\n\t"
      "s_waitcnt lgkmcnt(0)\n\t"
      "s_barrier\n\t"
      "v_mfma_f32_16x16x32_bf16 %1, v[232:235], v[220:223], %1\n\t"
      "ds_read_b128 v[184:187], v160 offset:0\n\t"
      "v_mfma_f32_16x16x32_bf16 %8, v[232:235], v[224:227], %8\n\t"
      "ds_read_b128 v[188:191], v160 offset:2048\n\t"
      "v_mfma_f32_16x16x32_bf16 %2, v[236:239], v[220:223], %2\n\t"
      "ds_read_b128 v[192:195], v162 offset:0\n\t"
      "v_mfma_f32_16x16x32_bf16 %9, v[236:239], v[224:227], %9\n\t"
      "ds_read_b128 v[196:199], v162 offset:1024\n\t"
      "v_mfma_f32_16x16x32_bf16 %3, v[240:243], v[220:223], %3\n\t"
      "ds_read_b128 v[200:203], v162 offset:2048\n\t"
      "v_mfma_f32_16x16x32_bf16 %10, v[240:243], v[224:227], %10\n\t"
      "ds_read_b128 v[204:207], v162 offset:3072\n\t"
      "v_mfma_f32_16x16x32_bf16 %4, v[244:247], v[220:223], %4\n\t"
      "ds_read_b128 v[208:211], v162 offset:4096\n\t"
      "v_mfma_f32_16x16x32_bf16 %11, v[244:247], v[224:227], %11\n\t"
      "ds_read_b128 v[212:215], v162 offset:5120\n\t"
      "v_mfma_f32_16x16x32_bf16 %5, v[248:251], v[220:223], %5\n\t"
      "ds_read_b128 v[216:219], v162 offset:6144\n\t"
      "v_mfma_f32_16x16x32_bf16 %12, v[248:251], v[224:227], %12\n\t"
      "v_mfma_f32_16x16x32_bf16 %6, v[252:255], v[220:223], %6\n\t"
      "s_cmp_eq_u32 %14, 0\n\t"
      "v_mfma_f32_16x16x32_bf16 %13, v[252:255], v[224:227], %13\n\t"
      "s_cbranch_scc1 2f\n\t"
      "s_waitcnt lgkmcnt(5)\n\t"
      "v_mfma_f32_16x16x32_bf16 %0, v[192:195], v[184:187], %0\n\t"
      "ds_read_b128 v[220:223], v161 offset:0\n\t"
      "v_mfma_f32_16x16x32_bf16 %7, v[192:195], v[188:191], %7\n\t"
      "ds_read_b128 v[224:227], v161 offset:2048\n\t"
      "v_mfma_f32_16x16x32_bf16 %1, v[196:199], v[184:187], %1\n\t"
      "ds_read_b128 v[228:231], v162 offset:7168\n\t"
      "v_mfma_f32_16x16x32_bf16 %8, v[196:199], v[188:191], %8\n\t"
      "ds_read_b128 v[232:235], v162 offset:8192\n\t"
      "s_waitcnt lgkmcnt(7)\n\t"
      "v_mfma_f32_16x16x32_bf16 %2, v[200:203], v[184:187], %2\n\t"
      "ds_read_b128 v[236:239], v162 offset:9216\n\t"
      "v_mfma_f32_16x16x32_bf16 %9, v[200:203], v[188:191], %9\n\t"
      "ds_read_b128 v[240:243], v162 offset:10240\n\t"
      "v_mfma_f32_16x16x32_bf16 %3, v[204:207], v[184:187], %3\n\t"
      "ds_read_b128 v[244:247], v162 offset:11264\n\t"
      "v_mfma_f32_16x16x32_bf16 %10, v[204:207], v[188:191], %10\n\t"
      "ds_read_b128 v[248:251], v162 offset:12288\n\t"
      "s_waitcnt lgkmcnt(9)\n\t"
      "v_mfma_f32_16x16x32_bf16 %4, v[208:211], v[184:187], %4\n\t"
      "ds_read_b128 v[252:255], v162 offset:13312\n\t"
      "v_mfma_f32_16x16x32_bf16 %11, v[208:211], v[188:191], %11\n\t"
      "v_mfma_f32_16x16x32_bf16 %5, v[212:215], v[184:187], %5\n\t"
      "v_mfma_f32_16x16x32_bf16 %12, v[212:215], v[188:191], %12\n\t"
      "s_waitcnt lgkmcnt(9)\n\t"
      "v_mfma_f32_16x16x32_bf16 %6, v[216:219], v[184:187], %6\n\t"
      "s_sub_u32 %14, %14, 1\n\t"
      "v_mfma_f32_16x16x32_bf16 %13, v[216:219], v[188:191], %13\n\t"
      "s_waitcnt lgkmcnt(5)\n\t"
      "v_mfma_f32_16x16x32_bf16 %0, v[228:231], v[220:223], %0\n\t"
      "v_mfma_f32_16x16x32_bf16 %7, v[228:231], v[224:227], %7\n\t"
      "s_waitcnt lgkmcnt(0)\n\t"
      "s_barrier\n\t"
      "v_mfma_f32_16x16x32_bf16 %1, v[232:235], v[220:223], %1\n\t"
      "ds_read_b128 v[184:187], v160 offset:32768\n\t"
      "v_mfma_f32_16x16x32_bf16 %8, v[232:235], v[224:227], %8\n\t"
      "ds_read_b128 v[188:191], v160 offset:34816\n\t"
      "v_mfma_f32_16x16x32_bf16 %2, v[236:239], v[220:223], %2\n\t"
      "ds_read_b128 v[192:195], v162 offset:32768\n\t"
      "v_mfma_f32_16x16x32_bf16 %9, v[236:239], v[224:227], %9\n\t"
      "ds_read_b128 v[196:199], v162 offset:33792\n\t"
      "v_mfma_f32_16x16x32_bf16 %3, v[240:243], v[220:223], %3\n\t"
      "ds_read_b128 v[200:203], v162 offset:34816\n\t"
      "v_mfma_f32_16x16x32_bf16 %10, v[240:243], v[224:227], %10\n\t"
      "ds_read_b128 v[204:207], v162 offset:35840\n\t"
      "v_mfma_f32_16x16x32_bf16 %4, v[244:247], v[220:223], %4\n\t"
      "ds_read_b128 v[208:211], v162 offset:36864\n\t"
      "v_mfma_f32_16x16x32_bf16 %11, v[244:247], v[224:227], %11\n\t"
      "ds_read_b128 v[212:215], v162 offset:37888\n\t"
      "v_mfma_f32_16x16x32_bf16 %5, v[248:251], v[220:223], %5\n\t"
      "ds_read_b128 v[216:219], v162 offset:38912\n\t"
      "v_mfma_f32_16x16x32_bf16 %12, v[248:251], v[224:227], %12\n\t"
      "v_mfma_f32_16x16x32_bf16 %6, v[252:255], v[220:223], %6\n\t"
      "s_cmp_eq_u32 %14, 0\n\t"
      "v_mfma_f32_16x16x32_bf16 %13, v[252:255], v[224:227], %13\n\t"
      "s_cbranch_scc1 2f\n\t"
      "s_waitcnt lgkmcnt(5)\n\t"
      "v_mfma_f32_16x16x32_bf16 %0, v[192:195], v[184:187], %0\n\t"
      "ds_read_b128 v[220:223], v161 offset:32768\n\t"
      "v_mfma_f32_16x16x32_bf16 %7, v[192:195], v[188:191], %7\n\t"
      "ds_read_b128 v[224:227], v161 offset:34816\n\t"
      "v_mfma_f32_16x16x32_bf16 %1, v[196:199], v[184:187], %1\n\t"
      "ds_read_b128 v[228:231], v162 offset:39936\n\t"
      "v_mfma_f32_16x16x32_bf16 %8, v[196:199], v[188:191], %8\n\t"
      "ds_read_b128 v[232:235], v162 offset:40960\n\t"
      "s_waitcnt lgkmcnt(7)\n\t"
      "v_mfma_f32_16x16x32_bf16 %2, v[200:203], v[184:187], %2\n\t"
      "ds_read_b128 v[236:239], v162 offset:41984\n\t"
      "v_mfma_f32_16x16x32_bf16 %9, v[200:203], v[188:191], %9\n\t"
      "ds_read_b128 v[240:243], v162 offset:43008\n\t"
      "v_mfma_f32_16x16x32_bf16 %3, v[204:207], v[184:187], %3\n\t"
      "ds_read_b128 v[244:247], v162 offset:44032\n\t"
      "v_mfma_f32_16x16x32_bf16 %10, v[204:207], v[188:191], %10\n\t"
      "ds_read_b128 v[248:251], v162 offset:45056\n\t"
      "s_waitcnt lgkmcnt(9)\n\t"
      "v_mfma_f32_16x16x32_bf16 %4, v[208:211], v[184:187], %4\n\t"
      "ds_read_b128 v[252:255], v162 offset:46080\n\t"
      "v_mfma_f32_16x16x32_bf16 %11, v[208:211], v[188:191], %11\n\t"
      "v_mfma_f32_16x16x32_bf16 %5, v[212:215], v[184:187], %5\n\t"
      "v_mfma_f32_16x16x32_bf16 %12, v[212:215], v[188:191], %12\n\t"
      "s_waitcnt lgkmcnt(9)\n\t"
      "v_mfma_f32_16x16x32_bf16 %6, v[216:219], v[184:187], %6\n\t"
      "s_sub_u32 %14, %14, 1\n\t"
      "v_mfma_f32_16x16x32_bf16 %13, v[216:219], v[188:191], %13\n\t"
      "s_waitcnt lgkmcnt(5)\n\t"
      "v_mfma_f32_16x16x32_bf16 %0, v[228:231], v[220:223], %0\n\t"
      "v_mfma_f32_16x16x32_bf16 %7, v[228:231], v[224:227], %7\n\t"
      "s_waitcnt lgkmcnt(0)\n\t"
      "s_waitcnt vmcnt(0)\n\t"
      "s_barrier\n\t"
      "v_mfma_f32_16x16x32_bf16 %1, v[232:235], v[220:223], %1\n\t"
      "ds_read_b128 v[184:187], v163 offset:0\n\t"
      "v_mfma_f32_16x16x32_bf16 %8, v[232:235], v[224:227], %8\n\t"
      "ds_read_b128 v[188:191], v163 offset:2048\n\t"
      "v_mfma_f32_16x16x32_bf16 %2, v[236:239], v[220:223], %2\n\t"
      "ds_read_b128 v[192:195], v165 offset:0\n\t"
      "v_mfma_f32_16x16x32_bf16 %9, v[236:239], v[224:227], %9\n\t"
      "ds_read_b128 v[196:199], v165 offset:1024\n\t"
      "v_mfma_f32_16x16x32_bf16 %3, v[240:243], v[220:223], %3\n\t"
      "ds_read_b128 v[200:203], v165 offset:2048\n\t"
      "v_mfma_f32_16x16x32_bf16 %10, v[240:243], v[224:227], %10\n\t"
      "ds_read_b128 v[204:207], v165 offset:3072\n\t"
      "v_mfma_f32_16x16x32_bf16 %4, v[244:247], v[220:223], %4\n\t"
      "ds_read_b128 v[208:211], v165 offset:4096\n\t"
      "v_mfma_f32_16x16x32_bf16 %11, v[244:247], v[224:227], %11\n\t"
      "ds_read_b128 v[212:215], v165 offset:5120\n\t"
      "v_mfma_f32_16x16x32_bf16 %5, v[248:251], v[220:223], %5\n\t"
      "ds_read_b128 v[216:219], v165 offset:6144\n\t"
      "v_mfma_f32_16x16x32_bf16 %12, v[248:251], v[224:227], %12\n\t"
      "v_mfma_f32_16x16x32_bf16 %6, v[252:255], v[220:223], %6\n\t"
      "s_cmp_eq_u32 %14, 0\n\t"
      "v_mfma_f32_16x16x32_bf16 %13, v[252:255], v[224:227], %13\n\t"
      "s_cbranch_scc1 2f\n\t"
      "s_waitcnt lgkmcnt(5)\n\t"
      "v_mfma_f32_16x16x32_bf16 %0, v[192:195], v[184:187], %0\n\t"
      "ds_read_b128 v[220:223], v164 offset:0\n\t"
      "v_mfma_f32_16x16x32_bf16 %7, v[192:195], v[188:191], %7\n\t"
      "ds_read_b128 v[224:227], v164 offset:2048\n\t"
      "v_mfma_f32_16x16x32_bf16 %1, v[196:199], v[184:187], %1\n\t"
      "ds_read_b128 v[228:231], v165 offset:7168\n\t"
      "v_mfma_f32_16x16x32_bf16 %8, v[196:199], v[188:191], %8\n\t"
      "ds_read_b128 v[232:235], v165 offset:8192\n\t"
      "s_waitcnt lgkmcnt(7)\n\t"
      "v_mfma_f32_16x16x32_bf16 %2, v[200:203], v[184:187], %2\n\t"
      "ds_read_b128 v[236:239], v165 offset:9216\n\t"
      "v_mfma_f32_16x16x32_bf16 %9, v[200:203], v[188:191], %9\n\t"
      "ds_read_b128 v[240:243], v165 offset:10240\n\t"
      "v_mfma_f32_16x16x32_bf16 %3, v[204:207], v[184:187], %3\n\t"
      "ds_read_b128 v[244:247], v165 offset:11264\n\t"
      "v_mfma_f32_16x16x32_bf16 %10, v[204:207], v[188:191], %10\n\t"
      "ds_read_b128 v[248:251], v165 offset:12288\n\t"
      "s_waitcnt lgkmcnt(9)\n\t"
      "v_mfma_f32_16x16x32_bf16 %4, v[208:211], v[184:187], %4\n\t"
      "ds_read_b128 v[252:255], v165 offset:13312\n\t"
      "v_mfma_f32_16x16x32_bf16 %11, v[208:211], v[188:191], %11\n\t"
      "v_mfma_f32_16x16x32_bf16 %5, v[212:215], v[184:187], %5\n\t"
      "v_mfma_f32_16x16x32_bf16 %12, v[212:215], v[188:191], %12\n\t"
      "s_waitcnt lgkmcnt(9)\n\t"
      "v_mfma_f32_16x16x32_bf16 %6, v[216:219], v[184:187], %6\n\t"
      "s_sub_u32 %14, %14, 1\n\t"
      "v_mfma_f32_16x16x32_bf16 %13, v[216:219], v[188:191], %13\n\t"
      "s_waitcnt lgkmcnt(5)\n\t"
      "v_mfma_f32_16x16x32_bf16 %0, v[228:231], v[220:223], %0\n\t"
      "v_mfma_f32_16x16x32_bf16 %7, v[228:231], v[224:227], %7\n\t"
      "s_waitcnt lgkmcnt(0)\n\t"
      "s_barrier\n\t"
      "v_mfma_f32_16x16x32_bf16 %1, v[232:235], v[220:223], %1\n\t"
      "ds_read_b128 v[184:187], v157 offset:0\n\t"
      "v_mfma_f32_16x16x32_bf16 %8, v[232:235], v[224:227], %8\n\t"
      "ds_read_b128 v[188:191], v157 offset:2048\n\t"
      "v_mfma_f32_16x16x32_bf16 %2, v[236:239], v[220:223], %2\n\t"
      "ds_read_b128 v[192:195], v159 offset:0\n\t"
      "v_mfma_f32_16x16x32_bf16 %9, v[236:239], v[224:227], %9\n\t"
      "ds_read_b128 v[196:199], v159 offset:1024\n\t"
      "v_mfma_f32_16x16x32_bf16 %3, v[240:243], v[220:223], %3\n\t"
      "ds_read_b128 v[200:203], v159 offset:2048\n\t"
      "v_mfma_f32_16x16x32_bf16 %10, v[240:243], v[224:227], %10\n\t"
      "ds_read_b128 v[204:207], v159 offset:3072\n\t"
      "v_mfma_f32_16x16x32_bf16 %4, v[244:247], v[220:223], %4\n\t"
      "ds_read_b128 v[208:211], v159 offset:4096\n\t"
      "v_mfma_f32_16x16x32_bf16 %11, v[244:247], v[224:227], %11\n\t"
      "ds_read_b128 v[212:215], v159 offset:5120\n\t"
      "v_mfma_f32_16x16x32_bf16 %5, v[248:251], v[220:223], %5\n\t"
      "ds_read_b128 v[216:219], v159 offset:6144\n\t"
      "v_mfma_f32_16x16x32_bf16 %12, v[248:251], v[224:227], %12\n\t"
      "v_mfma_f32_16x16x32_bf16 %6, v[252:255], v[220:223], %6\n\t"
      "s_cmp_eq_u32 %14, 0\n\t"
      "v_mfma_f32_16x16x32_bf16 %13, v[252:255], v[224:227], %13\n\t"
      "s_cbranch_scc0 1b\n\t"
      "2:\n\t"
      "s_waitcnt vmcnt(0) lgkmcnt(0)\n\t"
      "s_nop 7\n\t"
      "s_nop 1\n\t"
      : "+v"(acc[0][0]), "+v"(acc[0][1]), "+v"(acc[0][2]), "+v"(acc[0][3]), "+v"(acc[0][4]), "+v"(acc[0][5]), "+v"(acc[0][6]), "+v"(acc[1][0]), "+v"(acc[1][1]), "+v"(acc[1][2]), "+v"(acc[1][3]), "+v"(acc[1][4]), "+v"(acc[1][5]), "+v"(acc[1][6]), "+s"(n), "=&v"(zz[0][0]), "=&v"(zz[0][1]), "=&v"(zz[0][2]), "=&v"(zz[0][3]), "=&v"(zz[1][0]), "=&v"(zz[1][1]), "=&v"(zz[1][2]), "=&v"(zz[1][3])
      : "v"(a0), "v"(b0), "v"(zvoff), "s"(zbase)
      : "memory", "scc", "s90", "s91", "v157", "v158", "v159", "v160", "v161", "v162", "v163", "v164", "v165", "v166", "v184", "v185", "v186", "v187", "v188", "v189", "v190", "v191", "v192", "v193", "v194", "v195", "v196", "v197", "v198", "v199", "v200", "v201", "v202", "v203", "v204", "v205", "v206", "v207", "v208", "v209", "v210", "v211", "v212", "v213", "v214", "v215", "v216", "v217", "v218", "v219", "v220", "v221", "v222", "v223", "v224", "v225", "v226", "v227", "v228", "v229", "v230", "v231", "v232", "v233", "v234", "v235", "v236", "v237", "v238", "v239", "v240", "v241", "v242", "v243", "v244", "v245", "v246", "v247", "v248", "v249", "v250", "v251", "v252", "v253", "v254", "v255");
}
DI void pass_z2(f32x4 (&acc)[2][4], u32 a0, u32 trbase, u32 f5) {
  asm volatile(
      "v_mov_b32 v198, %8\n\t"
      "v_xor_b32 v199, 64, %8\n\t"
      "v_add_u32 v200, 0x10000, v198\n\t"
      "v_add_u32 v201, 0x10000, v199\n\t"
      "v_mov_b32 v202, %10\n\t"
      "v_add_u32 v202, v202, %9\n\t"
      "v_xor_b32 v203, 32, %10\n\t"
      "v_add_u32 v203, v203, %9\n\t"
      "v_xor_b32 v204, 64, %10\n\t"
      "v_add_u32 v204, v204, %9\n\t"
      "v_xor_b32 v205, 96, %10\n\t"
      "v_add_u32 v205, v205, %9\n\t"
      "ds_read_b128 v[208:211], v198 offset:0\n\t"
      "ds_read_b128 v[212:215], v198 offset:2048\n\t"
      "ds_read_b64_tr_b16 v[216:217], v202 offset:0\n\t"
      "ds_read_b64_tr_b16 v[218:219], v202 offset:512\n\t"
      "ds_read_b64_tr_b16 v[220:221], v203 offset:0\n\t"
      "ds_read_b64_tr_b16 v[222:223], v203 offset:512\n\t"
      "ds_read_b64_tr_b16 v[224:225], v204 offset:0\n\t"
      "ds_read_b64_tr_b16 v[226:227], v204 offset:512\n\t"
      "ds_read_b64_tr_b16 v[228:229], v205 offset:0\n\t"
      "ds_read_b64_tr_b16 v[230:231], v205 offset:512\n\t"
      "s_waitcnt lgkmcnt(4)\n\t"
      "v_mfma_f32_16x16x32_bf16 %0, v[208:211], v[216:219], %0\n\t"
      "ds_read_b128 v[232:235], v199 offset:0\n\t"
      "ds_read_b128 v[236:239], v199 offset:2048\n\t"
      "v_mfma_f32_16x16x32_bf16 %4, v[212:215], v[216:219], %4\n\t"
      "ds_read_b64_tr_b16 v[240:241], v202 offset:4096\n\t"
      "ds_read_b64_tr_b16 v[242:243], v202 offset:4608\n\t"
      "v_mfma_f32_16x16x32_bf16 %1, v[208:211], v[220:223], %1\n\t"
      "ds_read_b64_tr_b16 v[244:245], v203 offset:4096\n\t"
      "ds_read_b64_tr_b16 v[246:247], v203 offset:4608\n\t"
      "v_mfma_f32_16x16x32_bf16 %5, v[212:215], v[220:223], %5\n\t"
      "ds_read_b64_tr_b16 v[248:249], v204 offset:4096\n\t"
      "ds_read_b64_tr_b16 v[250:251], v204 offset:4608\n\t"
      "s_waitcnt lgkmcnt(8)\n\t"
      "v_mfma_f32_16x16x32_bf16 %2, v[208:211], v[224:227], %2\n\t"
      "ds_read_b64_tr_b16 v[252:253], v205 offset:4096\n\t"
      "v_mfma_f32_16x16x32_bf16 %6, v[212:215], v[224:227], %6\n\t"
      "ds_read_b64_tr_b16 v[254:255], v205 offset:4608\n\t"
      "v_mfma_f32_16x16x32_bf16 %3, v[208:211], v[228:231], %3\n\t"
      "v_mfma_f32_16x16x32_bf16 %7, v[212:215], v[228:231], %7\n\t"
      "s_barrier\n\t"
      "s_waitcnt lgkmcnt(4)\n\t"
      "v_mfma_f32_16x16x32_bf16 %0, v[232:235], v[240:243], %0\n\t"
      "ds_read_b128 v[208:211], v198 offset:16384\n\t"
      "ds_read_b128 v[212:215], v198 offset:18432\n\t"
      "v_mfma_f32_16x16x32_bf16 %4, v[236:239], v[240:243], %4\n\t"
      "ds_read_b64_tr_b16 v[216:217], v202 offset:8192\n\t"
      "ds_read_b64_tr_b16 v[218:219], v202 offset:8704\n\t"
      "v_mfma_f32_16x16x32_bf16 %1, v[232:235], v[244:247], %1\n\t"
      "ds_read_b64_tr_b16 v[220:221], v203 offset:8192\n\t"
      "ds_read_b64_tr_b16 v[222:223], v203 offset:8704\n\t"
      "v_mfma_f32_16x16x32_bf16 %5, v[236:239], v[244:247], %5\n\t"
      "ds_read_b64_tr_b16 v[224:225], v204 offset:8192\n\t"
      "ds_read_b64_tr_b16 v[226:227], v204 offset:8704\n\t"
      "s_waitcnt lgkmcnt(8)\n\t"
      "v_mfma_f32_16x16x32_bf16 %2, v[232:235], v[248:251], %2\n\t"
      "ds_read_b64_tr_b16 v[228:229], v205 offset:8192\n\t"
      "v_mfma_f32_16x16x32_bf16 %6, v[236:239], v[248:251], %6\n\t"
      "ds_read_b64_tr_b16 v[230:231], v205 offset:8704\n\t"
      "v_mfma_f32_16x16x32_bf16 %3, v[232:235], v[252:255], %3\n\t"
      "v_mfma_f32_16x16x32_bf16 %7, v[236:239], v[252:255], %7\n\t"
      "s_waitcnt lgkmcnt(4)\n\t"
      "v_mfma_f32_16x16x32_bf16 %0, v[208:211], v[216:219], %0\n\t"
      "ds_read_b128 v[232:235], v199 offset:16384\n\t"
      "ds_read_b128 v[236:239], v199 offset:18432\n\t"
      "v_mfma_f32_16x16x32_bf16 %4, v[212:215], v[216:219], %4\n\t"
      "ds_read_b64_tr_b16 v[240:241], v202 offset:12288\n\t"
      "ds_read_b64_tr_b16 v[242:243], v202 offset:12800\n\t"
      "v_mfma_f32_16x16x32_bf16 %1, v[208:211], v[220:223], %1\n\t"
      "ds_read_b64_tr_b16 v[244:245], v203 offset:12288\n\t"
      "ds_read_b64_tr_b16 v[246:247], v203 offset:12800\n\t"
      "v_mfma_f32_16x16x32_bf16 %5, v[212:215], v[220:223], %5\n\t"
      "ds_read_b64_tr_b16 v[248:249], v204 offset:12288\n\t"
      "ds_read_b64_tr_b16 v[250:251], v204 offset:12800\n\t"
      "s_waitcnt lgkmcnt(8)\n\t"
      "v_mfma_f32_16x16x32_bf16 %2, v[208:211], v[224:227], %2\n\t"
      "ds_read_b64_tr_b16 v[252:253], v205 offset:12288\n\t"
      "v_mfma_f32_16x16x32_bf16 %6, v[212:215], v[224:227], %6\n\t"
      "ds_read_b64_tr_b16 v[254:255], v205 offset:12800\n\t"
      "v_mfma_f32_16x16x32_bf16 %3, v[208:211], v[228:231], %3\n\t"
      "v_mfma_f32_16x16x32_bf16 %7, v[212:215], v[228:231], %7\n\t"
      "s_barrier\n\t"
      "s_waitcnt lgkmcnt(4)\n\t"
      "v_mfma_f32_16x16x32_bf16 %0, v[232:235], v[240:243], %0\n\t"
      "ds_read_b128 v[208:211], v198 offset:32768\n\t"
      "ds_read_b128 v[212:215], v198 offset:34816\n\t"
      "v_mfma_f32_16x16x32_bf16 %4, v[236:239], v[240:243], %4\n\t"
      "ds_read_b64_tr_b16 v[216:217], v202 offset:16384\n\t"
      "ds_read_b64_tr_b16 v[218:219], v202 offset:16896\n\t"
      "v_mfma_f32_16x16x32_bf16 %1, v[232:235], v[244:247], %1\n\t"
      "ds_read_b64_tr_b16 v[220:221], v203 offset:16384\n\t"
      "ds_read_b64_tr_b16 v[222:223], v203 offset:16896\n\t"
      "v_mfma_f32_16x16x32_bf16 %5, v[236:239], v[244:247], %5\n\t"
      "ds_read_b64_tr_b16 v[224:225], v204 offset:16384\n\t"
      "ds_read_b64_tr_b16 v[226:227], v204 offset:16896\n\t"
      "s_waitcnt lgkmcnt(8)\n\t"
      "v_mfma_f32_16x16x32_bf16 %2, v[232:235], v[248:251], %2\n\t"
      "ds_read_b64_tr_b16 v[228:229], v205 offset:16384\n\t"
      "v_mfma_f32_16x16x32_bf16 %6, v[236:239], v[248:251], %6\n\t"
      "ds_read_b64_tr_b16 v[230:231], v205 offset:16896\n\t"
      "v_mfma_f32_16x16x32_bf16 %3, v[232:235], v[252:255], %3\n\t"
      "v_mfma_f32_16x16x32_bf16 %7, v[236:239], v[252:255], %7\n\t"
      "s_waitcnt lgkmcnt(4)\n\t"
      "v_mfma_f32_16x16x32_bf16 %0, v[208:211], v[216:219], %0\n\t"
      "ds_read_b128 v[232:235], v199 offset:32768\n\t"
      "ds_read_b128 v[236:239], v199 offset:34816\n\t"
      "v_mfma_f32_16x16x32_bf16 %4, v[212:215], v[216:219], %4\n\t"
      "ds_read_b64_tr_b16 v[240:241], v202 offset:20480\n\t"
      "ds_read_b64_tr_b16 v[242:243], v202 offset:20992\n\t"
      "v_mfma_f32_16x16x32_bf16 %1, v[208:211], v[220:223], %1\n\t"
      "ds_read_b64_tr_b16 v[244:245], v203 offset:20480\n\t"
      "ds_read_b64_tr_b16 v[246:247], v203 offset:20992\n\t"
      "v_mfma_f32_16x16x32_bf16 %5, v[212:215], v[220:223], %5\n\t"
      "ds_read_b64_tr_b16 v[248:249], v204 offset:20480\n\t"
      "ds_read_b64_tr_b16 v[250:251], v204 offset:20992\n\t"
      "s_waitcnt lgkmcnt(8)\n\t"
      "v_mfma_f32_16x16x32_bf16 %2, v[208:211], v[224:227], %2\n\t"
      "ds_read_b64_tr_b16 v[252:253], v205 offset:20480\n\t"
      "v_mfma_f32_16x16x32_bf16 %6, v[212:215], v[224:227], %6\n\t"
      "ds_read_b64_tr_b16 v[254:255], v205 offset:20992\n\t"
      "v_mfma_f32_16x16x32_bf16 %3, v[208:211], v[228:231], %3\n\t"
      "v_mfma_f32_16x16x32_bf16 %7, v[212:215], v[228:231], %7\n\t"
      "s_barrier\n\t"
      "s_waitcnt lgkmcnt(4)\n\t"
      "v_mfma_f32_16x16x32_bf16 %0, v[232:235], v[240:243], %0\n\t"
      "ds_read_b128 v[208:211], v198 offset:49152\n\t"
      "ds_read_b128 v[212:215], v198 offset:51200\n\t"
      "v_mfma_f32_16x16x32_bf16 %4, v[236:239], v[240:243], %4\n\t"
      "ds_read_b64_tr_b16 v[216:217], v202 offset:24576\n\t"
      "ds_read_b64_tr_b16 v[218:219], v202 offset:25088\n\t"
      "v_mfma_f32_16x16x32_bf16 %1, v[232:235], v[244:247], %1\n\t"
      "ds_read_b64_tr_b16 v[220:221], v203 offset:24576\n\t"
      "ds_read_b64_tr_b16 v[222:223], v203 offset:25088\n\t"
      "v_mfma_f32_16x16x32_bf16 %5, v[236:239], v[244:247], %5\n\t"
      "ds_read_b64_tr_b16 v[224:225], v204 offset:24576\n\t"
      "ds_read_b64_tr_b16 v[226:227], v204 offset:25088\n\t"
      "s_waitcnt lgkmcnt(8)\n\t"
      "v_mfma_f32_16x16x32_bf16 %2, v[232:235], v[248:251], %2\n\t"
      "ds_read_b64_tr_b16 v[228:229], v205 offset:24576\n\t"
      "v_mfma_f32_16x16x32_bf16 %6, v[236:239], v[248:251], %6\n\t"
      "ds_read_b64_tr_b16 v[230:231], v205 offset:25088\n\t"
      "v_mfma_f32_16x16x32_bf16 %3, v[232:235], v[252:255], %3\n\t"
      "v_mfma_f32_16x16x32_bf16 %7, v[236:239], v[252:255], %7\n\t"
      "s_waitcnt lgkmcnt(4)\n\t"
      "v_mfma_f32_16x16x32_bf16 %0, v[208:211], v[216:219], %0\n\t"
      "ds_read_b128 v[232:235], v199 offset:49152\n\t"
      "ds_read_b128 v[236:239], v199 offset:51200\n\t"
      "v_mfma_f32_16x16x32_bf16 %4, v[212:215], v[216:219], %4\n\t"
      "ds_read_b64_tr_b16 v[240:241], v202 offset:28672\n\t"
      "ds_read_b64_tr_b16 v[242:243], v202 offset:29184\n\t"
      "v_mfma_f32_16x16x32_bf16 %1, v[208:211], v[220:223], %1\n\t"
      "ds_read_b64_tr_b16 v[244:245], v203 offset:28672\n\t"
      "ds_read_b64_tr_b16 v[246:247], v203 offset:29184\n\t"
      "v_mfma_f32_16x16x32_bf16 %5, v[212:215], v[220:223], %5\n\t"
      "ds_read_b64_tr_b16 v[248:249], v204 offset:28672\n\t"
      "ds_read_b64_tr_b16 v[250:251], v204 offset:29184\n\t"
      "s_waitcnt lgkmcnt(8)\n\t"
      "v_mfma_f32_16x16x32_bf16 %2, v[208:211], v[224:227], %2\n\t"
      "ds_read_b64_tr_b16 v[252:253], v205 offset:28672\n\t"
      "v_mfma_f32_16x16x32_bf16 %6, v[212:215], v[224:227], %6\n\t"
      "ds_read_b64_tr_b16 v[254:255], v205 offset:29184\n\t"
      "v_mfma_f32_16x16x32_bf16 %3, v[208:211], v[228:231], %3\n\t"
      "v_mfma_f32_16x16x32_bf16 %7, v[212:215], v[228:231], %7\n\t"
      "s_barrier\n\t"
      "s_waitcnt lgkmcnt(4)\n\t"
      "v_mfma_f32_16x16x32_bf16 %0, v[232:235], v[240:243], %0\n\t"
      "ds_read_b128 v[208:211], v200 offset:0\n\t"
      "ds_read_b128 v[212:215], v200 offset:2048\n\t"
      "v_mfma_f32_16x16x32_bf16 %4, v[236:239], v[240:243], %4\n\t"
      "ds_read_b64_tr_b16 v[216:217], v202 offset:32768\n\t"
      "ds_read_b64_tr_b16 v[218:219], v202 offset:33280\n\t"
      "v_mfma_f32_16x16x32_bf16 %1, v[232:235], v[244:247], %1\n\t"
      "ds_read_b64_tr_b16 v[220:221], v203 offset:32768\n\t"
      "ds_read_b64_tr_b16 v[222:223], v203 offset:33280\n\t"
      "v_mfma_f32_16x16x32_bf16 %5, v[236:239], v[244:247], %5\n\t"
      "ds_read_b64_tr_b16 v[224:225], v204 offset:32768\n\t"
      "ds_read_b64_tr_b16 v[226:227], v204 offset:33280\n\t"
      "s_waitcnt lgkmcnt(8)\n\t"
      "v_mfma_f32_16x16x32_bf16 %2, v[232:235], v[248:251], %2\n\t"
      "ds_read_b64_tr_b16 v[228:229], v205 offset:32768\n\t"
      "v_mfma_f32_16x16x32_bf16 %6, v[236:239], v[248:251], %6\n\t"
      "ds_read_b64_tr_b16 v[230:231], v205 offset:33280\n\t"
      "v_mfma_f32_16x16x32_bf16 %3, v[232:235], v[252:255], %3\n\t"
      "v_mfma_f32_16x16x32_bf16 %7, v[236:239], v[252:255], %7\n\t"
      "s_waitcnt lgkmcnt(4)\n\t"
      "v_mfma_f32_16x16x32_bf16 %0, v[208:211], v[216:219], %0\n\t"
      "ds_read_b128 v[232:235], v201 offset:0\n\t"
      "ds_read_b128 v[236:239], v201 offset:2048\n\t"
      "v_mfma_f32_16x16x32_bf16 %4, v[212:215], v[216:219], %4\n\t"
      "ds_read_b64_tr_b16 v[240:241], v202 offset:36864\n\t"
      "ds_read_b64_tr_b16 v[242:243], v202 offset:37376\n\t"
      "v_mfma_f32_16x16x32_bf16 %1, v[208:211], v[220:223], %1\n\t"
      "ds_read_b64_tr_b16 v[244:245], v203 offset:36864\n\t"
      "ds_read_b64_tr_b16 v[246:247], v203 offset:37376\n\t"
      "v_mfma_f32_16x16x32_bf16 %5, v[212:215], v[220:223], %5\n\t"
      "ds_read_b64_tr_b16 v[248:249], v204 offset:36864\n\t"
      "ds_read_b64_tr_b16 v[250:251], v204 offset:37376\n\t"
      "s_waitcnt lgkmcnt(8)\n\t"
      "v_mfma_f32_16x16x32_bf16 %2, v[208:211], v[224:227], %2\n\t"
      "ds_read_b64_tr_b16 v[252:253], v205 offset:36864\n\t"
      "v_mfma_f32_16x16x32_bf16 %6, v[212:215], v[224:227], %6\n\t"
      "ds_read_b64_tr_b16 v[254:255], v205 offset:37376\n\t"
      "v_mfma_f32_16x16x32_bf16 %3, v[208:211], v[228:231], %3\n\t"
      "v_mfma_f32_16x16x32_bf16 %7, v[212:215], v[228:231], %7\n\t"
      "s_barrier\n\t"
      "s_waitcnt lgkmcnt(4)\n\t"
      "v_mfma_f32_16x16x32_bf16 %0, v[232:235], v[240:243], %0\n\t"
      "ds_read_b128 v[208:211], v200 offset:16384\n\t"
      "ds_read_b128 v[212:215], v200 offset:18432\n\t"
      "v_mfma_f32_16x16x32_bf16 %4, v[236:239], v[240:243], %4\n\t"
      "ds_read_b64_tr_b16 v[216:217], v202 offset:0\n\t"
      "ds_read_b64_tr_b16 v[218:219], v202 offset:512\n\t"
      "v_mfma_f32_16x16x32_bf16 %1, v[232:235], v[244:247], %1\n\t"
      "ds_read_b64_tr_b16 v[220:221], v203 offset:0\n\t"
      "ds_read_b64_tr_b16 v[222:223], v203 offset:512\n\t"
      "v_mfma_f32_16x16x32_bf16 %5, v[236:239], v[244:247], %5\n\t"
      "ds_read_b64_tr_b16 v[224:225], v204 offset:0\n\t"
      "ds_read_b64_tr_b16 v[226:227], v204 offset:512\n\t"
      "s_waitcnt lgkmcnt(8)\n\t"
      "v_mfma_f32_16x16x32_bf16 %2, v[232:235], v[248:251], %2\n\t"
      "ds_read_b64_tr_b16 v[228:229], v205 offset:0\n\t"
      "v_mfma_f32_16x16x32_bf16 %6, v[236:239], v[248:251], %6\n\t"
      "ds_read_b64_tr_b16 v[230:231], v205 offset:512\n\t"
      "v_mfma_f32_16x16x32_bf16 %3, v[232:235], v[252:255], %3\n\t"
      "v_mfma_f32_16x16x32_bf16 %7, v[236:239], v[252:255], %7\n\t"
      "s_waitcnt lgkmcnt(4)\n\t"
      "v_mfma_f32_16x16x32_bf16 %0, v[208:211], v[216:219], %0\n\t"
      "ds_read_b128 v[232:235], v201 offset:16384\n\t"
      "ds_read_b128 v[236:239], v201 offset:18432\n\t"
      "v_mfma_f32_16x16x32_bf16 %4, v[212:215], v[216:219], %4\n\t"
      "ds_read_b64_tr_b16 v[240:241], v202 offset:4096\n\t"
      "ds_read_b64_tr_b16 v[242:243], v202 offset:4608\n\t"
      "v_mfma_f32_16x16x32_bf16 %1, v[208:211], v[220:223], %1\n\t"
      "ds_read_b64_tr_b16 v[244:245], v203 offset:4096\n\t"
      "ds_read_b64_tr_b16 v[246:247], v203 offset:4608\n\t"
      "v_mfma_f32_16x16x32_bf16 %5, v[212:215], v[220:223], %5\n\t"
      "ds_read_b64_tr_b16 v[248:249], v204 offset:4096\n\t"
      "ds_read_b64_tr_b16 v[250:251], v204 offset:4608\n\t"
      "s_waitcnt lgkmcnt(8)\n\t"
      "v_mfma_f32_16x16x32_bf16 %2, v[208:211], v[224:227], %2\n\t"
      "ds_read_b64_tr_b16 v[252:253], v205 offset:4096\n\t"
      "v_mfma_f32_16x16x32_bf16 %6, v[212:215], v[224:227], %6\n\t"
      "ds_read_b64_tr_b16 v[254:255], v205 offset:4608\n\t"
      "v_mfma_f32_16x16x32_bf16 %3, v[208:211], v[228:231], %3\n\t"
      "v_mfma_f32_16x16x32_bf16 %7, v[212:215], v[228:231], %7\n\t"
      "s_barrier\n\t"
      "s_waitcnt lgkmcnt(4)\n\t"
      "v_mfma_f32_16x16x32_bf16 %0, v[232:235], v[240:243], %0\n\t"
      "ds_read_b128 v[208:211], v200 offset:32768\n\t"
      "ds_read_b128 v[212:215], v200 offset:34816\n\t"
      "v_mfma_f32_16x16x32_bf16 %4, v[236:239], v[240:243], %4\n\t"
      "ds_read_b64_tr_b16 v[216:217], v202 offset:8192\n\t"
      "ds_read_b64_tr_b16 v[218:219], v202 offset:8704\n\t"
      "v_mfma_f32_16x16x32_bf16 %1, v[232:235], v[244:247], %1\n\t"
      "ds_read_b64_tr_b16 v[220:221], v203 offset:8192\n\t"
      "ds_read_b64_tr_b16 v[222:223], v203 offset:8704\n\t"
      "v_mfma_f32_16x16x32_bf16 %5, v[236:239], v[244:247], %5\n\t"
      "ds_read_b64_tr_b16 v[224:225], v204 offset:8192\n\t"
      "ds_read_b64_tr_b16 v[226:227], v204 offset:8704\n\t"
      "s_waitcnt lgkmcnt(8)\n\t"
      "v_mfma_f32_16x16x32_bf16 %2, v[232:235], v[248:251], %2\n\t"
      "ds_read_b64_tr_b16 v[228:229], v205 offset:8192\n\t"
      "v_mfma_f32_16x16x32_bf16 %6, v[236:239], v[248:251], %6\n\t"
      "ds_read_b64_tr_b16 v[230:231], v205 offset:8704\n\t"
      "v_mfma_f32_16x16x32_bf16 %3, v[232:235], v[252:255], %3\n\t"
      "v_mfma_f32_16x16x32_bf16 %7, v[236:239], v[252:255], %7\n\t"
      "s_waitcnt lgkmcnt(4)\n\t"
      "v_mfma_f32_16x16x32_bf16 %0, v[208:211], v[216:219], %0\n\t"
      "ds_read_b128 v[232:235], v201 offset:32768\n\t"
      "ds_read_b128 v[236:239], v201 offset:34816\n\t"
      "v_mfma_f32_16x16x32_bf16 %4, v[212:215], v[216:219], %4\n\t"
      "ds_read_b64_tr_b16 v[240:241], v202 offset:12288\n\t"
      "ds_read_b64_tr_b16 v[242:243], v202 offset:12800\n\t"
      "v_mfma_f32_16x16x32_bf16 %1, v[208:211], v[220:223], %1\n\t"
      "ds_read_b64_tr_b16 v[244:245], v203 offset:12288\n\t"
      "ds_read_b64_tr_b16 v[246:247], v203 offset:12800\n\t"
      "v_mfma_f32_16x16x32_bf16 %5, v[212:215], v[220:223], %5\n\t"
      "ds_read_b64_tr_b16 v[248:249], v204 offset:12288\n\t"
      "ds_read_b64_tr_b16 v[250:251], v204 offset:12800\n\t"
      "s_waitcnt lgkmcnt(8)\n\t"
      "v_mfma_f32_16x16x32_bf16 %2, v[208:211], v[224:227], %2\n\t"
      "ds_read_b64_tr_b16 v[252:253], v205 offset:12288\n\t"
      "v_mfma_f32_16x16x32_bf16 %6, v[212:215], v[224:227], %6\n\t"
      "ds_read_b64_tr_b16 v[254:255], v205 offset:12800\n\t"
      "v_mfma_f32_16x16x32_bf16 %3, v[208:211], v[228:231], %3\n\t"
      "v_mfma_f32_16x16x32_bf16 %7, v[212:215], v[228:231], %7\n\t"
      "s_barrier\n\t"
      "s_waitcnt lgkmcnt(4)\n\t"
      "v_mfma_f32_16x16x32_bf16 %0, v[232:235], v[240:243], %0\n\t"
      "v_mfma_f32_16x16x32_bf16 %4, v[236:239], v[240:243], %4\n\t"
      "v_mfma_f32_16x16x32_bf16 %1, v[232:235], v[244:247], %1\n\t"
      "v_mfma_f32_16x16x32_bf16 %5, v[236:239], v[244:247], %5\n\t"
      "s_waitcnt lgkmcnt(0)\n\t"
      "v_mfma_f32_16x16x32_bf16 %2, v[232:235], v[248:251], %2\n\t"
      "v_mfma_f32_16x16x32_bf16 %6, v[236:239], v[248:251], %6\n\t"
      "v_mfma_f32_16x16x32_bf16 %3, v[232:235], v[252:255], %3\n\t"
      "v_mfma_f32_16x16x32_bf16 %7, v[236:239], v[252:255], %7\n\t"
      "s_nop 7\n\t"
      "s_nop 1\n\t"
      : "+v"(acc[0][0]), "+v"(acc[0][1]), "+v"(acc[0][2]), "+v"(acc[0][3]), "+v"(acc[1][0]), "+v"(acc[1][1]), "+v"(acc[1][2]), "+v"(acc[1][3])
      : "v"(a0), "v"(trbase), "v"(f5)
      : "memory", "v198", "v199", "v200", "v201", "v202", "v203", "v204", "v205", "v206", "v207", "v208", "v209", "v210", "v211", "v212", "v213", "v214", "v215", "v216", "v217", "v218", "v219", "v220", "v221", "v222", "v223", "v224", "v225", "v226", "v227", "v228", "v229", "v230", "v231", "v232", "v233", "v234", "v235", "v236", "v237", "v238", "v239", "v240", "v241", "v242", "v243", "v244", "v245", "v246", "v247", "v248", "v249", "v250", "v251", "v252", "v253", "v254", "v255");
}
DI void pass_ab(f32x4 (&acc)[2][4], u32 a0, u32 d0) {
  asm volatile(
      "v_mov_b32 v198, %8\n\t"
      "v_xor_b32 v199, 64, %8\n\t"
      "v_add_u32 v200, 0x10000, v198\n\t"
      "v_add_u32 v201, 0x10000, v199\n\t"
      "v_mov_b32 v202, %9\n\t"
      "v_xor_b32 v203, 64, %9\n\t"
      "ds_read_b128 v[208:211], v198 offset:0\n\t"
      "ds_read_b128 v[212:215], v198 offset:2048\n\t"
      "ds_read_b128 v[216:219], v202 offset:16384\n\t"
      "ds_read_b128 v[220:223], v202 offset:18432\n\t"
      "ds_read_b128 v[224:227], v202 offset:20480\n\t"
      "ds_read_b128 v[228:231], v202 offset:22528\n\t"
      "s_waitcnt lgkmcnt(2)\n\t"
      "v_mfma_f32_16x16x32_bf16 %0, v[208:211], v[216:219], %0\n\t"
      "ds_read_b128 v[232:235], v199 offset:0\n\t"
      "v_mfma_f32_16x16x32_bf16 %4, v[212:215], v[216:219], %4\n\t"
      "ds_read_b128 v[236:239], v199 offset:2048\n\t"
      "v_mfma_f32_16x16x32_bf16 %1, v[208:211], v[220:223], %1\n\t"
      "ds_read_b128 v[240:243], v203 offset:16384\n\t"
      "v_mfma_f32_16x16x32_bf16 %5, v[212:215], v[220:223], %5\n\t"
      "ds_read_b128 v[244:247], v203 offset:18432\n\t"
      "s_waitcnt lgkmcnt(4)\n\t"
      "v_mfma_f32_16x16x32_bf16 %2, v[208:211], v[224:227], %2\n\t"
      "ds_read_b128 v[248:251], v203 offset:20480\n\t"
      "v_mfma_f32_16x16x32_bf16 %6, v[212:215], v[224:227], %6\n\t"
      "ds_read_b128 v[252:255], v203 offset:22528\n\t"
      "v_mfma_f32_16x16x32_bf16 %3, v[208:211], v[228:231], %3\n\t"
      "v_mfma_f32_16x16x32_bf16 %7, v[212:215], v[228:231], %7\n\t"
      "s_barrier\n\t"
      "s_waitcnt lgkmcnt(2)\n\t"
      "v_mfma_f32_16x16x32_bf16 %0, v[232:235], v[240:243], %0\n\t"
      "ds_read_b128 v[208:211], v198 offset:16384\n\t"
      "v_mfma_f32_16x16x32_bf16 %4, v[236:239], v[240:243], %4\n\t"
      "ds_read_b128 v[212:215], v198 offset:18432\n\t"
      "v_mfma_f32_16x16x32_bf16 %1, v[232:235], v[244:247], %1\n\t"
      "ds_read_b128 v[216:219], v202 offset:24576\n\t"
      "v_mfma_f32_16x16x32_bf16 %5, v[236:239], v[244:247], %5\n\t"
      "ds_read_b128 v[220:223], v202 offset:26624\n\t"
      "s_waitcnt lgkmcnt(4)\n\t"
      "v_mfma_f32_16x16x32_bf16 %2, v[232:235], v[248:251], %2\n\t"
      "ds_read_b128 v[224:227], v202 offset:28672\n\t"
      "v_mfma_f32_16x16x32_bf16 %6, v[236:239], v[248:251], %6\n\t"
      "ds_read_b128 v[228:231], v202 offset:30720\n\t"
      "v_mfma_f32_16x16x32_bf16 %3, v[232:235], v[252:255], %3\n\t"
      "v_mfma_f32_16x16x32_bf16 %7, v[236:239], v[252:255], %7\n\t"
      "s_waitcnt lgkmcnt(2)\n\t"
      "v_mfma_f32_16x16x32_bf16 %0, v[208:211], v[216:219], %0\n\t"
      "ds_read_b128 v[232:235], v199 offset:16384\n\t"
      "v_mfma_f32_16x16x32_bf16 %4, v[212:215], v[216:219], %4\n\t"
      "ds_read_b128 v[236:239], v199 offset:18432\n\t"
      "v_mfma_f32_16x16x32_bf16 %1, v[208:211], v[220:223], %1\n\t"
      "ds_read_b128 v[240:243], v203 offset:24576\n\t"
      "v_mfma_f32_16x16x32_bf16 %5, v[212:215], v[220:223], %5\n\t"
      "ds_read_b128 v[244:247], v203 offset:26624\n\t"
      "s_waitcnt lgkmcnt(4)\n\t"
      "v_mfma_f32_16x16x32_bf16 %2, v[208:211], v[224:227], %2\n\t"
      "ds_read_b128 v[248:251], v203 offset:28672\n\t"
      "v_mfma_f32_16x16x32_bf16 %6, v[212:215], v[224:227], %6\n\t"
      "ds_read_b128 v[252:255], v203 offset:30720\n\t"
      "v_mfma_f32_16x16x32_bf16 %3, v[208:211], v[228:231], %3\n\t"
      "v_mfma_f32_16x16x32_bf16 %7, v[212:215], v[228:231], %7\n\t"
      "s_barrier\n\t"
      "s_waitcnt lgkmcnt(2)\n\t"
      "v_mfma_f32_16x16x32_bf16 %0, v[232:235], v[240:243], %0\n\t"
      "ds_read_b128 v[208:211], v198 offset:32768\n\t"
      "v_mfma_f32_16x16x32_bf16 %4, v[236:239], v[240:243], %4\n\t"
      "ds_read_b128 v[212:215], v198 offset:34816\n\t"
      "v_mfma_f32_16x16x32_bf16 %1, v[232:235], v[244:247], %1\n\t"
      "ds_read_b128 v[216:219], v202 offset:32768\n\t"
      "v_mfma_f32_16x16x32_bf16 %5, v[236:239], v[244:247], %5\n\t"
      "ds_read_b128 v[220:223], v202 offset:34816\n\t"
      "s_waitcnt lgkmcnt(4)\n\t"
      "v_mfma_f32_16x16x32_bf16 %2, v[232:235], v[248:251], %2\n\t"
      "ds_read_b128 v[224:227], v202 offset:36864\n\t"
      "v_mfma_f32_16x16x32_bf16 %6, v[236:239], v[248:251], %6\n\t"
      "ds_read_b128 v[228:231], v202 offset:38912\n\t"
      "v_mfma_f32_16x16x32_bf16 %3, v[232:235], v[252:255], %3\n\t"
      "v_mfma_f32_16x16x32_bf16 %7, v[236:239], v[252:255], %7\n\t"
      "s_waitcnt lgkmcnt(2)\n\t"
      "v_mfma_f32_16x16x32_bf16 %0, v[208:211], v[216:219], %0\n\t"
      "ds_read_b128 v[232:235], v199 offset:32768\n\t"
      "v_mfma_f32_16x16x32_bf16 %4, v[212:215], v[216:219], %4\n\t"
      "ds_read_b128 v[236:239], v199 offset:34816\n\t"
      "v_mfma_f32_16x16x32_bf16 %1, v[208:211], v[220:223], %1\n\t"
      "ds_read_b128 v[240:243], v203 offset:32768\n\t"
      "v_mfma_f32_16x16x32_bf16 %5, v[212:215], v[220:223], %5\n\t"
      "ds_read_b128 v[244:247], v203 offset:34816\n\t"
      "s_waitcnt lgkmcnt(4)\n\t"
      "v_mfma_f32_16x16x32_bf16 %2, v[208:211], v[224:227], %2\n\t"
      "ds_read_b128 v[248:251], v203 offset:36864\n\t"
      "v_mfma_f32_16x16x32_bf16 %6, v[212:215], v[224:227], %6\n\t"
      "ds_read_b128 v[252:255], v203 offset:38912\n\t"
      "v_mfma_f32_16x16x32_bf16 %3, v[208:211], v[228:231], %3\n\t"
      "v_mfma_f32_16x16x32_bf16 %7, v[212:215], v[228:231], %7\n\t"
      "s_barrier\n\t"
      "s_waitcnt lgkmcnt(2)\n\t"
      "v_mfma_f32_16x16x32_bf16 %0, v[232:235], v[240:243], %0\n\t"
      "ds_read_b128 v[208:211], v198 offset:49152\n\t"
      "v_mfma_f32_16x16x32_bf16 %4, v[236:239], v[240:243], %4\n\t"
      "ds_read_b128 v[212:215], v198 offset:51200\n\t"
      "v_mfma_f32_16x16x32_bf16 %1, v[232:235], v[244:247], %1\n\t"
      "ds_read_b128 v[216:219], v202 offset:0\n\t"
      "v_mfma_f32_16x16x32_bf16 %5, v[236:239], v[244:247], %5\n\t"
      "ds_read_b128 v[220:223], v202 offset:2048\n\t"
      "s_waitcnt lgkmcnt(4)\n\t"
      "v_mfma_f32_16x16x32_bf16 %2, v[232:235], v[248:251], %2\n\t"
      "ds_read_b128 v[224:227], v202 offset:4096\n\t"
      "v_mfma_f32_16x16x32_bf16 %6, v[236:239], v[248:251], %6\n\t"
      "ds_read_b128 v[228:231], v202 offset:6144\n\t"
      "v_mfma_f32_16x16x32_bf16 %3, v[232:235], v[252:255], %3\n\t"
      "v_mfma_f32_16x16x32_bf16 %7, v[236:239], v[252:255], %7\n\t"
      "s_waitcnt lgkmcnt(2)\n\t"
      "v_mfma_f32_16x16x32_bf16 %0, v[208:211], v[216:219], %0\n\t"
      "ds_read_b128 v[232:235], v199 offset:49152\n\t"
      "v_mfma_f32_16x16x32_bf16 %4, v[212:215], v[216:219], %4\n\t"
      "ds_read_b128 v[236:239], v199 offset:51200\n\t"
      "v_mfma_f32_16x16x32_bf16 %1, v[208:211], v[220:223], %1\n\t"
      "ds_read_b128 v[240:243], v203 offset:0\n\t"
      "v_mfma_f32_16x16x32_bf16 %5, v[212:215], v[220:223], %5\n\t"
      "ds_read_b128 v[244:247], v203 offset:2048\n\t"
      "s_waitcnt lgkmcnt(4)\n\t"
      "v_mfma_f32_16x16x32_bf16 %2, v[208:211], v[224:227], %2\n\t"
      "ds_read_b128 v[248:251], v203 offset:4096\n\t"
      "v_mfma_f32_16x16x32_bf16 %6, v[212:215], v[224:227], %6\n\t"
      "ds_read_b128 v[252:255], v203 offset:6144\n\t"
      "v_mfma_f32_16x16x32_bf16 %3, v[208:211], v[228:231], %3\n\t"
      "v_mfma_f32_16x16x32_bf16 %7, v[212:215], v[228:231], %7\n\t"
      "s_barrier\n\t"
      "s_waitcnt lgkmcnt(2)\n\t"
      "v_mfma_f32_16x16x32_bf16 %0, v[232:235], v[240:243], %0\n\t"
      "ds_read_b128 v[208:211], v200 offset:0\n\t"
      "v_mfma_f32_16x16x32_bf16 %4, v[236:239], v[240:243], %4\n\t"
      "ds_read_b128 v[212:215], v200 offset:2048\n\t"
      "v_mfma_f32_16x16x32_bf16 %1, v[232:235], v[244:247], %1\n\t"
      "ds_read_b128 v[216:219], v202 offset:8192\n\t"
      "v_mfma_f32_16x16x32_bf16 %5, v[236:239], v[244:247], %5\n\t"
      "ds_read_b128 v[220:223], v202 offset:10240\n\t"
      "s_waitcnt lgkmcnt(4)\n\t"
      "v_mfma_f32_16x16x32_bf16 %2, v[232:235], v[248:251], %2\n\t"
      "ds_read_b128 v[224:227], v202 offset:12288\n\t"
      "v_mfma_f32_16x16x32_bf16 %6, v[236:239], v[248:251], %6\n\t"
      "ds_read_b128 v[228:231], v202 offset:14336\n\t"
      "v_mfma_f32_16x16x32_bf16 %3, v[232:235], v[252:255], %3\n\t"
      "v_mfma_f32_16x16x32_bf16 %7, v[236:239], v[252:255], %7\n\t"
      "s_waitcnt lgkmcnt(2)\n\t"
      "v_mfma_f32_16x16x32_bf16 %0, v[208:211], v[216:219], %0\n\t"
      "ds_read_b128 v[232:235], v201 offset:0\n\t"
      "v_mfma_f32_16x16x32_bf16 %4, v[212:215], v[216:219], %4\n\t"
      "ds_read_b128 v[236:239], v201 offset:2048\n\t"
      "v_mfma_f32_16x16x32_bf16 %1, v[208:211], v[220:223], %1\n\t"
      "ds_read_b128 v[240:243], v203 offset:8192\n\t"
      "v_mfma_f32_16x16x32_bf16 %5, v[212:215], v[220:223], %5\n\t"
      "ds_read_b128 v[244:247], v203 offset:10240\n\t"
      "s_waitcnt lgkmcnt(4)\n\t"
      "v_mfma_f32_16x16x32_bf16 %2, v[208:211], v[224:227], %2\n\t"
      "ds_read_b128 v[248:251], v203 offset:12288\n\t"
      "v_mfma_f32_16x16x32_bf16 %6, v[212:215], v[224:227], %6\n\t"
      "ds_read_b128 v[252:255], v203 offset:14336\n\t"
      "v_mfma_f32_16x16x32_bf16 %3, v[208:211], v[228:231], %3\n\t"
      "v_mfma_f32_16x16x32_bf16 %7, v[212:215], v[228:231], %7\n\t"
      "s_barrier\n\t"
      "s_waitcnt lgkmcnt(2)\n\t"
      "v_mfma_f32_16x16x32_bf16 %0, v[232:235], v[240:243], %0\n\t"
      "ds_read_b128 v[208:211], v200 offset:16384\n\t"
      "v_mfma_f32_16x16x32_bf16 %4, v[236:239], v[240:243], %4\n\t"
      "ds_read_b128 v[212:215], v200 offset:18432\n\t"
      "v_mfma_f32_16x16x32_bf16 %1, v[232:235], v[244:247], %1\n\t"
      "ds_read_b128 v[216:219], v202 offset:16384\n\t"
      "v_mfma_f32_16x16x32_bf16 %5, v[236:239], v[244:247], %5\n\t"
      "ds_read_b128 v[220:223], v202 offset:18432\n\t"
      "s_waitcnt lgkmcnt(4)\n\t"
      "v_mfma_f32_16x16x32_bf16 %2, v[232:235], v[248:251], %2\n\t"
      "ds_read_b128 v[224:227], v202 offset:20480\n\t"
      "v_mfma_f32_16x16x32_bf16 %6, v[236:239], v[248:251], %6\n\t"
      "ds_read_b128 v[228:231], v202 offset:22528\n\t"
      "v_mfma_f32_16x16x32_bf16 %3, v[232:235], v[252:255], %3\n\t"
      "v_mfma_f32_16x16x32_bf16 %7, v[236:239], v[252:255], %7\n\t"
      "s_waitcnt lgkmcnt(2)\n\t"
      "v_mfma_f32_16x16x32_bf16 %0, v[208:211], v[216:219], %0\n\t"
      "ds_read_b128 v[232:235], v201 offset:16384\n\t"
      "v_mfma_f32_16x16x32_bf16 %4, v[212:215], v[216:219], %4\n\t"
      "ds_read_b128 v[236:239], v201 offset:18432\n\t"
      "v_mfma_f32_16x16x32_bf16 %1, v[208:211], v[220:223], %1\n\t"
      "ds_read_b128 v[240:243], v203 offset:16384\n\t"
      "v_mfma_f32_16x16x32_bf16 %5, v[212:215], v[220:223], %5\n\t"
      "ds_read_b128 v[244:247], v203 offset:18432\n\t"
      "s_waitcnt lgkmcnt(4)\n\t"
      "v_mfma_f32_16x16x32_bf16 %2, v[208:211], v[224:227], %2\n\t"
      "ds_read_b128 v[248:251], v203 offset:20480\n\t"
      "v_mfma_f32_16x16x32_bf16 %6, v[212:215], v[224:227], %6\n\t"
      "ds_read_b128 v[252:255], v203 offset:22528\n\t"
      "v_mfma_f32_16x16x32_bf16 %3, v[208:211], v[228:231], %3\n\t"
      "v_mfma_f32_16x16x32_bf16 %7, v[212:215], v[228:231], %7\n\t"
      "s_barrier\n\t"
      "s_waitcnt lgkmcnt(2)\n\t"
      "v_mfma_f32_16x16x32_bf16 %0, v[232:235], v[240:243], %0\n\t"
      "ds_read_b128 v[208:211], v200 offset:32768\n\t"
      "v_mfma_f32_16x16x32_bf16 %4, v[236:239], v[240:243], %4\n\t"
      "ds_read_b128 v[212:215], v200 offset:34816\n\t"
      "v_mfma_f32_16x16x32_bf16 %1, v[232:235], v[244:247], %1\n\t"
      "ds_read_b128 v[216:219], v202 offset:24576\n\t"
      "v_mfma_f32_16x16x32_bf16 %5, v[236:239], v[244:247], %5\n\t"
      "ds_read_b128 v[220:223], v202 offset:26624\n\t"
      "s_waitcnt lgkmcnt(4)\n\t"
      "v_mfma_f32_16x16x32_bf16 %2, v[232:235], v[248:251], %2\n\t"
      "ds_read_b128 v[224:227], v202 offset:28672\n\t"
      "v_mfma_f32_16x16x32_bf16 %6, v[236:239], v[248:251], %6\n\t"
      "ds_read_b128 v[228:231], v202 offset:30720\n\t"
      "v_mfma_f32_16x16x32_bf16 %3, v[232:235], v[252:255], %3\n\t"
      "v_mfma_f32_16x16x32_bf16 %7, v[236:239], v[252:255], %7\n\t"
      "s_waitcnt lgkmcnt(2)\n\t"
      "v_mfma_f32_16x16x32_bf16 %0, v[208:211], v[216:219], %0\n\t"
      "ds_read_b128 v[232:235], v201 offset:32768\n\t"
      "v_mfma_f32_16x16x32_bf16 %4, v[212:215], v[216:219], %4\n\t"
      "ds_read_b128 v[236:239], v201 offset:34816\n\t"
      "v_mfma_f32_16x16x32_bf16 %1, v[208:211], v[220:223], %1\n\t"
      "ds_read_b128 v[240:243], v203 offset:24576\n\t"
      "v_mfma_f32_16x16x32_bf16 %5, v[212:215], v[220:223], %5\n\t"
      "ds_read_b128 v[244:247], v203 offset:26624\n\t"
      "s_waitcnt lgkmcnt(4)\n\t"
      "v_mfma_f32_16x16x32_bf16 %2, v[208:211], v[224:227], %2\n\t"
      "ds_read_b128 v[248:251], v203 offset:28672\n\t"
      "v_mfma_f32_16x16x32_bf16 %6, v[212:215], v[224:227], %6\n\t"
      "ds_read_b128 v[252:255], v203 offset:30720\n\t"
      "v_mfma_f32_16x16x32_bf16 %3, v[208:211], v[228:231], %3\n\t"
      "v_mfma_f32_16x16x32_bf16 %7, v[212:215], v[228:231], %7\n\t"
      "s_barrier\n\t"
      "s_waitcnt lgkmcnt(2)\n\t"
      "v_mfma_f32_16x16x32_bf16 %0, v[232:235], v[240:243], %0\n\t"
      "v_mfma_f32_16x16x32_bf16 %4, v[236:239], v[240:243], %4\n\t"
      "v_mfma_f32_16x16x32_bf16 %1, v[232:235], v[244:247], %1\n\t"
      "v_mfma_f32_16x16x32_bf16 %5, v[236:239], v[244:247], %5\n\t"
      "s_waitcnt lgkmcnt(0)\n\t"
      "v_mfma_f32_16x16x32_bf16 %2, v[232:235], v[248:251], %2\n\t"
      "v_mfma_f32_16x16x32_bf16 %6, v[236:239], v[248:251], %6\n\t"
      "v_mfma_f32_16x16x32_bf16 %3, v[232:235], v[252:255], %3\n\t"
      "v_mfma_f32_16x16x32_bf16 %7, v[236:239], v[252:255], %7\n\t"
      "s_nop 7\n\t"
      "s_nop 1\n\t"
      : "+v"(acc[0][0]), "+v"(acc[0][1]), "+v"(acc[0][2]), "+v"(acc[0][3]), "+v"(acc[1][0]), "+v"(acc[1][1]), "+v"(acc[1][2]), "+v"(acc[1][3])
      : "v"(a0), "v"(d0)
      : "memory", "v198", "v199", "v200", "v201", "v202", "v203", "v204", "v205", "v206", "v207", "v208", "v209", "v210", "v211", "v212", "v213", "v214", "v215", "v216", "v217", "v218", "v219", "v220", "v221", "v222", "v223", "v224", "v225", "v226", "v227", "v228", "v229", "v230", "v231", "v232", "v233", "v234", "v235", "v236", "v237", "v238", "v239", "v240", "v241", "v242", "v243", "v244", "v245", "v246", "v247", "v248", "v249", "v250", "v251", "v252", "v253", "v254", "v255");
}
DI void pass_z1(f32x4 (&acc)[2][4], u32 a0, u32 trbase, u32 f5) {
  asm volatile(
      "v_mov_b32 v198, %8\n\t"
      "v_xor_b32 v199, 64, %8\n\t"
      "v_add_u32 v200, 0x10000, v198\n\t"
      "v_add_u32 v201, 0x10000, v199\n\t"
      "v_mov_b32 v202, %10\n\t"
      "v_add_u32 v202, v202, %9\n\t"
      "v_xor_b32 v203, 32, %10\n\t"
      "v_add_u32 v203, v203, %9\n\t"
      "v_xor_b32 v204, 64, %10\n\t"
      "v_add_u32 v204, v204, %9\n\t"
      "v_xor_b32 v205, 96, %10\n\t"
      "v_add_u32 v205, v205, %9\n\t"
      "ds_read_b128 v[208:211], v198 offset:0\n\t"
      "ds_read_b128 v[212:215], v198 offset:2048\n\t"
      "ds_read_b64_tr_b16 v[216:217], v202 offset:32768\n\t"
      "ds_read_b64_tr_b16 v[218:219], v202 offset:33280\n\t"
      "ds_read_b64_tr_b16 v[220:221], v203 offset:32768\n\t"
      "ds_read_b64_tr_b16 v[222:223], v203 offset:33280\n\t"
      "ds_read_b64_tr_b16 v[224:225], v204 offset:32768\n\t"
      "ds_read_b64_tr_b16 v[226:227], v204 offset:33280\n\t"
      "ds_read_b64_tr_b16 v[228:229], v205 offset:32768\n\t"
      "ds_read_b64_tr_b16 v[230:231], v205 offset:33280\n\t"
      "s_waitcnt lgkmcnt(4)\n\t"
      "v_mfma_f32_16x16x32_bf16 %0, v[216:219], v[208:211], %0\n\t"
      "ds_read_b128 v[232:235], v199 offset:0\n\t"
      "ds_read_b128 v[236:239], v199 offset:2048\n\t"
      "v_mfma_f32_16x16x32_bf16 %4, v[216:219], v[212:215], %4\n\t"
      "ds_read_b64_tr_b16 v[240:241], v202 offset:36864\n\t"
      "ds_read_b64_tr_b16 v[242:243], v202 offset:37376\n\t"
      "v_mfma_f32_16x16x32_bf16 %1, v[220:223], v[208:211], %1\n\t"
      "ds_read_b64_tr_b16 v[244:245], v203 offset:36864\n\t"
      "ds_read_b64_tr_b16 v[246:247], v203 offset:37376\n\t"
      "v_mfma_f32_16x16x32_bf16 %5, v[220:223], v[212:215], %5\n\t"
      "ds_read_b64_tr_b16 v[248:249], v204 offset:36864\n\t"
      "ds_read_b64_tr_b16 v[250:251], v204 offset:37376\n\t"
      "s_waitcnt lgkmcnt(8)\n\t"
      "v_mfma_f32_16x16x32_bf16 %2, v[224:227], v[208:211], %2\n\t"
      "ds_read_b64_tr_b16 v[252:253], v205 offset:36864\n\t"
      "v_mfma_f32_16x16x32_bf16 %6, v[224:227], v[212:215], %6\n\t"
      "ds_read_b64_tr_b16 v[254:255], v205 offset:37376\n\t"
      "v_mfma_f32_16x16x32_bf16 %3, v[228:231], v[208:211], %3\n\t"
      "v_mfma_f32_16x16x32_bf16 %7, v[228:231], v[212:215], %7\n\t"
      "s_barrier\n\t"
      "s_waitcnt lgkmcnt(4)\n\t"
      "v_mfma_f32_16x16x32_bf16 %0, v[240:243], v[232:235], %0\n\t"
      "ds_read_b128 v[208:211], v198 offset:16384\n\t"
      "ds_read_b128 v[212:215], v198 offset:18432\n\t"
      "v_mfma_f32_16x16x32_bf16 %4, v[240:243], v[236:239], %4\n\t"
      "ds_read_b64_tr_b16 v[216:217], v202 offset:0\n\t"
      "ds_read_b64_tr_b16 v[218:219], v202 offset:512\n\t"
      "v_mfma_f32_16x16x32_bf16 %1, v[244:247], v[232:235], %1\n\t"
      "ds_read_b64_tr_b16 v[220:221], v203 offset:0\n\t"
      "ds_read_b64_tr_b16 v[222:223], v203 offset:512\n\t"
      "v_mfma_f32_16x16x32_bf16 %5, v[244:247], v[236:239], %5\n\t"
      "ds_read_b64_tr_b16 v[224:225], v204 offset:0\n\t"
      "ds_read_b64_tr_b16 v[226:227], v204 offset:512\n\t"
      "s_waitcnt lgkmcnt(8)\n\t"
      "v_mfma_f32_16x16x32_bf16 %2, v[248:251], v[232:235], %2\n\t"
      "ds_read_b64_tr_b16 v[228:229], v205 offset:0\n\t"
      "v_mfma_f32_16x16x32_bf16 %6, v[248:251], v[236:239], %6\n\t"
      "ds_read_b64_tr_b16 v[230:231], v205 offset:512\n\t"
      "v_mfma_f32_16x16x32_bf16 %3, v[252:255], v[232:235], %3\n\t"
      "v_mfma_f32_16x16x32_bf16 %7, v[252:255], v[236:239], %7\n\t"
      "s_waitcnt lgkmcnt(4)\n\t"
      "v_mfma_f32_16x16x32_bf16 %0, v[216:219], v[208:211], %0\n\t"
      "ds_read_b128 v[232:235], v199 offset:16384\n\t"
      "ds_read_b128 v[236:239], v199 offset:18432\n\t"
      "v_mfma_f32_16x16x32_bf16 %4, v[216:219], v[212:215], %4\n\t"
      "ds_read_b64_tr_b16 v[240:241], v202 offset:4096\n\t"
      "ds_read_b64_tr_b16 v[242:243], v202 offset:4608\n\t"
      "v_mfma_f32_16x16x32_bf16 %1, v[220:223], v[208:211], %1\n\t"
      "ds_read_b64_tr_b16 v[244:245], v203 offset:4096\n\t"
      "ds_read_b64_tr_b16 v[246:247], v203 offset:4608\n\t"
      "v_mfma_f32_16x16x32_bf16 %5, v[220:223], v[212:215], %5\n\t"
      "ds_read_b64_tr_b16 v[248:249], v204 offset:4096\n\t"
      "ds_read_b64_tr_b16 v[250:251], v204 offset:4608\n\t"
      "s_waitcnt lgkmcnt(8)\n\t"
      "v_mfma_f32_16x16x32_bf16 %2, v[224:227], v[208:211], %2\n\t"
      "ds_read_b64_tr_b16 v[252:253], v205 offset:4096\n\t"
      "v_mfma_f32_16x16x32_bf16 %6, v[224:227], v[212:215], %6\n\t"
      "ds_read_b64_tr_b16 v[254:255], v205 offset:4608\n\t"
      "v_mfma_f32_16x16x32_bf16 %3, v[228:231], v[208:211], %3\n\t"
      "v_mfma_f32_16x16x32_bf16 %7, v[228:231], v[212:215], %7\n\t"
      "s_barrier\n\t"
      "s_waitcnt lgkmcnt(4)\n\t"
      "v_mfma_f32_16x16x32_bf16 %0, v[240:243], v[232:235], %0\n\t"
      "ds_read_b128 v[208:211], v198 offset:32768\n\t"
      "ds_read_b128 v[212:215], v198 offset:34816\n\t"
      "v_mfma_f32_16x16x32_bf16 %4, v[240:243], v[236:239], %4\n\t"
      "ds_read_b64_tr_b16 v[216:217], v202 offset:8192\n\t"
      "ds_read_b64_tr_b16 v[218:219], v202 offset:8704\n\t"
      "v_mfma_f32_16x16x32_bf16 %1, v[244:247], v[232:235], %1\n\t"
      "ds_read_b64_tr_b16 v[220:221], v203 offset:8192\n\t"
      "ds_read_b64_tr_b16 v[222:223], v203 offset:8704\n\t"
      "v_mfma_f32_16x16x32_bf16 %5, v[244:247], v[236:239], %5\n\t"
      "ds_read_b64_tr_b16 v[224:225], v204 offset:8192\n\t"
      "ds_read_b64_tr_b16 v[226:227], v204 offset:8704\n\t"
      "s_waitcnt lgkmcnt(8)\n\t"
      "v_mfma_f32_16x16x32_bf16 %2, v[248:251], v[232:235], %2\n\t"
      "ds_read_b64_tr_b16 v[228:229], v205 offset:8192\n\t"
      "v_mfma_f32_16x16x32_bf16 %6, v[248:251], v[236:239], %6\n\t"
      "ds_read_b64_tr_b16 v[230:231], v205 offset:8704\n\t"
      "v_mfma_f32_16x16x32_bf16 %3, v[252:255], v[232:235], %3\n\t"
      "v_mfma_f32_16x16x32_bf16 %7, v[252:255], v[236:239], %7\n\t"
      "s_waitcnt lgkmcnt(4)\n\t"
      "v_mfma_f32_16x16x32_bf16 %0, v[216:219], v[208:211], %0\n\t"
      "ds_read_b128 v[232:235], v199 offset:32768\n\t"
      "ds_read_b128 v[236:239], v199 offset:34816\n\t"
      "v_mfma_f32_16x16x32_bf16 %4, v[216:219], v[212:215], %4\n\t"
      "ds_read_b64_tr_b16 v[240:241], v202 offset:12288\n\t"
      "ds_read_b64_tr_b16 v[242:243], v202 offset:12800\n\t"
      "v_mfma_f32_16x16x32_bf16 %1, v[220:223], v[208:211], %1\n\t"
      "ds_read_b64_tr_b16 v[244:245], v203 offset:12288\n\t"
      "ds_read_b64_tr_b16 v[246:247], v203 offset:12800\n\t"
      "v_mfma_f32_16x16x32_bf16 %5, v[220:223], v[212:215], %5\n\t"
      "ds_read_b64_tr_b16 v[248:249], v204 offset:12288\n\t"
      "ds_read_b64_tr_b16 v[250:251], v204 offset:12800\n\t"
      "s_waitcnt lgkmcnt(8)\n\t"
      "v_mfma_f32_16x16x32_bf16 %2, v[224:227], v[208:211], %2\n\t"
      "ds_read_b64_tr_b16 v[252:253], v205 offset:12288\n\t"
      "v_mfma_f32_16x16x32_bf16 %6, v[224:227], v[212:215], %6\n\t"
      "ds_read_b64_tr_b16 v[254:255], v205 offset:12800\n\t"
      "v_mfma_f32_16x16x32_bf16 %3, v[228:231], v[208:211], %3\n\t"
      "v_mfma_f32_16x16x32_bf16 %7, v[228:231], v[212:215], %7\n\t"
      "s_barrier\n\t"
      "s_waitcnt lgkmcnt(4)\n\t"
      "v_mfma_f32_16x16x32_bf16 %0, v[240:243], v[232:235], %0\n\t"
      "ds_read_b128 v[208:211], v198 offset:49152\n\t"
      "ds_read_b128 v[212:215], v198 offset:51200\n\t"
      "v_mfma_f32_16x16x32_bf16 %4, v[240:243], v[236:239], %4\n\t"
      "ds_read_b64_tr_b16 v[216:217], v202 offset:16384\n\t"
      "ds_read_b64_tr_b16 v[218:219], v202 offset:16896\n\t"
      "v_mfma_f32_16x16x32_bf16 %1, v[244:247], v[232:235], %1\n\t"
      "ds_read_b64_tr_b16 v[220:221], v203 offset:16384\n\t"
      "ds_read_b64_tr_b16 v[222:223], v203 offset:16896\n\t"
      "v_mfma_f32_16x16x32_bf16 %5, v[244:247], v[236:239], %5\n\t"
      "ds_read_b64_tr_b16 v[224:225], v204 offset:16384\n\t"
      "ds_read_b64_tr_b16 v[226:227], v204 offset:16896\n\t"
      "s_waitcnt lgkmcnt(8)\n\t"
      "v_mfma_f32_16x16x32_bf16 %2, v[248:251], v[232:235], %2\n\t"
      "ds_read_b64_tr_b16 v[228:229], v205 offset:16384\n\t"
      "v_mfma_f32_16x16x32_bf16 %6, v[248:251], v[236:239], %6\n\t"
      "ds_read_b64_tr_b16 v[230:231], v205 offset:16896\n\t"
      "v_mfma_f32_16x16x32_bf16 %3, v[252:255], v[232:235], %3\n\t"
      "v_mfma_f32_16x16x32_bf16 %7, v[252:255], v[236:239], %7\n\t"
      "s_waitcnt lgkmcnt(4)\n\t"
      "v_mfma_f32_16x16x32_bf16 %0, v[216:219], v[208:211], %0\n\t"
      "ds_read_b128 v[232:235], v199 offset:49152\n\t"
      "ds_read_b128 v[236:239], v199 offset:51200\n\t"
      "v_mfma_f32_16x16x32_bf16 %4, v[216:219], v[212:215], %4\n\t"
      "ds_read_b64_tr_b16 v[240:241], v202 offset:20480\n\t"
      "ds_read_b64_tr_b16 v[242:243], v202 offset:20992\n\t"
      "v_mfma_f32_16x16x32_bf16 %1, v[220:223], v[208:211], %1\n\t"
      "ds_read_b64_tr_b16 v[244:245], v203 offset:20480\n\t"
      "ds_read_b64_tr_b16 v[246:247], v203 offset:20992\n\t"
      "v_mfma_f32_16x16x32_bf16 %5, v[220:223], v[212:215], %5\n\t"
      "ds_read_b64_tr_b16 v[248:249], v204 offset:20480\n\t"
      "ds_read_b64_tr_b16 v[250:251], v204 offset:20992\n\t"
      "s_waitcnt lgkmcnt(8)\n\t"
      "v_mfma_f32_16x16x32_bf16 %2, v[224:227], v[208:211], %2\n\t"
      "ds_read_b64_tr_b16 v[252:253], v205 offset:20480\n\t"
      "v_mfma_f32_16x16x32_bf16 %6, v[224:227], v[212:215], %6\n\t"
      "ds_read_b64_tr_b16 v[254:255], v205 offset:20992\n\t"
      "v_mfma_f32_16x16x32_bf16 %3, v[228:231], v[208:211], %3\n\t"
      "v_mfma_f32_16x16x32_bf16 %7, v[228:231], v[212:215], %7\n\t"
      "s_waitcnt vmcnt(0)\n\t"
      "s_barrier\n\t"
      "s_waitcnt lgkmcnt(4)\n\t"
      "v_mfma_f32_16x16x32_bf16 %0, v[240:243], v[232:235], %0\n\t"
      "ds_read_b128 v[208:211], v200 offset:0\n\t"
      "ds_read_b128 v[212:215], v200 offset:2048\n\t"
      "v_mfma_f32_16x16x32_bf16 %4, v[240:243], v[236:239], %4\n\t"
      "ds_read_b64_tr_b16 v[216:217], v202 offset:24576\n\t"
      "ds_read_b64_tr_b16 v[218:219], v202 offset:25088\n\t"
      "v_mfma_f32_16x16x32_bf16 %1, v[244:247], v[232:235], %1\n\t"
      "ds_read_b64_tr_b16 v[220:221], v203 offset:24576\n\t"
      "ds_read_b64_tr_b16 v[222:223], v203 offset:25088\n\t"
      "v_mfma_f32_16x16x32_bf16 %5, v[244:247], v[236:239], %5\n\t"
      "ds_read_b64_tr_b16 v[224:225], v204 offset:24576\n\t"
      "ds_read_b64_tr_b16 v[226:227], v204 offset:25088\n\t"
      "s_waitcnt lgkmcnt(8)\n\t"
      "v_mfma_f32_16x16x32_bf16 %2, v[248:251], v[232:235], %2\n\t"
      "ds_read_b64_tr_b16 v[228:229], v205 offset:24576\n\t"
      "v_mfma_f32_16x16x32_bf16 %6, v[248:251], v[236:239], %6\n\t"
      "ds_read_b64_tr_b16 v[230:231], v205 offset:25088\n\t"
      "v_mfma_f32_16x16x32_bf16 %3, v[252:255], v[232:235], %3\n\t"
      "v_mfma_f32_16x16x32_bf16 %7, v[252:255], v[236:239], %7\n\t"
      "s_waitcnt lgkmcnt(4)\n\t"
      "v_mfma_f32_16x16x32_bf16 %0, v[216:219], v[208:211], %0\n\t"
      "ds_read_b128 v[232:235], v201 offset:0\n\t"
      "ds_read_b128 v[236:239], v201 offset:2048\n\t"
      "v_mfma_f32_16x16x32_bf16 %4, v[216:219], v[212:215], %4\n\t"
      "ds_read_b64_tr_b16 v[240:241], v202 offset:28672\n\t"
      "ds_read_b64_tr_b16 v[242:243], v202 offset:29184\n\t"
      "v_mfma_f32_16x16x32_bf16 %1, v[220:223], v[208:211], %1\n\t"
      "ds_read_b64_tr_b16 v[244:245], v203 offset:28672\n\t"
      "ds_read_b64_tr_b16 v[246:247], v203 offset:29184\n\t"
      "v_mfma_f32_16x16x32_bf16 %5, v[220:223], v[212:215], %5\n\t"
      "ds_read_b64_tr_b16 v[248:249], v204 offset:28672\n\t"
      "ds_read_b64_tr_b16 v[250:251], v204 offset:29184\n\t"
      "s_waitcnt lgkmcnt(8)\n\t"
      "v_mfma_f32_16x16x32_bf16 %2, v[224:227], v[208:211], %2\n\t"
      "ds_read_b64_tr_b16 v[252:253], v205 offset:28672\n\t"
      "v_mfma_f32_16x16x32_bf16 %6, v[224:227], v[212:215], %6\n\t"
      "ds_read_b64_tr_b16 v[254:255], v205 offset:29184\n\t"
      "v_mfma_f32_16x16x32_bf16 %3, v[228:231], v[208:211], %3\n\t"
      "v_mfma_f32_16x16x32_bf16 %7, v[228:231], v[212:215], %7\n\t"
      "s_barrier\n\t"
      "s_waitcnt lgkmcnt(4)\n\t"
      "v_mfma_f32_16x16x32_bf16 %0, v[240:243], v[232:235], %0\n\t"
      "ds_read_b128 v[208:211], v200 offset:16384\n\t"
      "ds_read_b128 v[212:215], v200 offset:18432\n\t"
      "v_mfma_f32_16x16x32_bf16 %4, v[240:243], v[236:239], %4\n\t"
      "ds_read_b64_tr_b16 v[216:217], v202 offset:32768\n\t"
      "ds_read_b64_tr_b16 v[218:219], v202 offset:33280\n\t"
      "v_mfma_f32_16x16x32_bf16 %1, v[244:247], v[232:235], %1\n\t"
      "ds_read_b64_tr_b16 v[220:221], v203 offset:32768\n\t"
      "ds_read_b64_tr_b16 v[222:223], v203 offset:33280\n\t"
      "v_mfma_f32_16x16x32_bf16 %5, v[244:247], v[236:239], %5\n\t"
      "ds_read_b64_tr_b16 v[224:225], v204 offset:32768\n\t"
      "ds_read_b64_tr_b16 v[226:227], v204 offset:33280\n\t"
      "s_waitcnt lgkmcnt(8)\n\t"
      "v_mfma_f32_16x16x32_bf16 %2, v[248:251], v[232:235], %2\n\t"
      "ds_read_b64_tr_b16 v[228:229], v205 offset:32768\n\t"
      "v_mfma_f32_16x16x32_bf16 %6, v[248:251], v[236:239], %6\n\t"
      "ds_read_b64_tr_b16 v[230:231], v205 offset:33280\n\t"
      "v_mfma_f32_16x16x32_bf16 %3, v[252:255], v[232:235], %3\n\t"
      "v_mfma_f32_16x16x32_bf16 %7, v[252:255], v[236:239], %7\n\t"
      "s_waitcnt lgkmcnt(4)\n\t"
      "v_mfma_f32_16x16x32_bf16 %0, v[216:219], v[208:211], %0\n\t"
      "ds_read_b128 v[232:235], v201 offset:16384\n\t"
      "ds_read_b128 v[236:239], v201 offset:18432\n\t"
      "v_mfma_f32_16x16x32_bf16 %4, v[216:219], v[212:215], %4\n\t"
      "ds_read_b64_tr_b16 v[240:241], v202 offset:36864\n\t"
      "ds_read_b64_tr_b16 v[242:243], v202 offset:37376\n\t"
      "v_mfma_f32_16x16x32_bf16 %1, v[220:223], v[208:211], %1\n\t"
      "ds_read_b64_tr_b16 v[244:245], v203 offset:36864\n\t"
      "ds_read_b64_tr_b16 v[246:247], v203 offset:37376\n\t"
      "v_mfma_f32_16x16x32_bf16 %5, v[220:223], v[212:215], %5\n\t"
      "ds_read_b64_tr_b16 v[248:249], v204 offset:36864\n\t"
      "ds_read_b64_tr_b16 v[250:251], v204 offset:37376\n\t"
      "s_waitcnt lgkmcnt(8)\n\t"
      "v_mfma_f32_16x16x32_bf16 %2, v[224:227], v[208:211], %2\n\t"
      "ds_read_b64_tr_b16 v[252:253], v205 offset:36864\n\t"
      "v_mfma_f32_16x16x32_bf16 %6, v[224:227], v[212:215], %6\n\t"
      "ds_read_b64_tr_b16 v[254:255], v205 offset:37376\n\t"
      "v_mfma_f32_16x16x32_bf16 %3, v[228:231], v[208:211], %3\n\t"
      "v_mfma_f32_16x16x32_bf16 %7, v[228:231], v[212:215], %7\n\t"
      "s_barrier\n\t"
      "s_waitcnt lgkmcnt(4)\n\t"
      "v_mfma_f32_16x16x32_bf16 %0, v[240:243], v[232:235], %0\n\t"
      "ds_read_b128 v[208:211], v200 offset:32768\n\t"
      "ds_read_b128 v[212:215], v200 offset:34816\n\t"
      "v_mfma_f32_16x16x32_bf16 %4, v[240:243], v[236:239], %4\n\t"
      "ds_read_b64_tr_b16 v[216:217], v202 offset:0\n\t"
      "ds_read_b64_tr_b16 v[218:219], v202 offset:512\n\t"
      "v_mfma_f32_16x16x32_bf16 %1, v[244:247], v[232:235], %1\n\t"
      "ds_read_b64_tr_b16 v[220:221], v203 offset:0\n\t"
      "ds_read_b64_tr_b16 v[222:223], v203 offset:512\n\t"
      "v_mfma_f32_16x16x32_bf16 %5, v[244:247], v[236:239], %5\n\t"
      "ds_read_b64_tr_b16 v[224:225], v204 offset:0\n\t"
      "ds_read_b64_tr_b16 v[226:227], v204 offset:512\n\t"
      "s_waitcnt lgkmcnt(8)\n\t"
      "v_mfma_f32_16x16x32_bf16 %2, v[248:251], v[232:235], %2\n\t"
      "ds_read_b64_tr_b16 v[228:229], v205 offset:0\n\t"
      "v_mfma_f32_16x16x32_bf16 %6, v[248:251], v[236:239], %6\n\t"
      "ds_read_b64_tr_b16 v[230:231], v205 offset:512\n\t"
      "v_mfma_f32_16x16x32_bf16 %3, v[252:255], v[232:235], %3\n\t"
      "v_mfma_f32_16x16x32_bf16 %7, v[252:255], v[236:239], %7\n\t"
      "s_waitcnt lgkmcnt(4)\n\t"
      "v_mfma_f32_16x16x32_bf16 %0, v[216:219], v[208:211], %0\n\t"
      "ds_read_b128 v[232:235], v201 offset:32768\n\t"
      "ds_read_b128 v[236:239], v201 offset:34816\n\t"
      "v_mfma_f32_16x16x32_bf16 %4, v[216:219], v[212:215], %4\n\t"
      "ds_read_b64_tr_b16 v[240:241], v202 offset:4096\n\t"
      "ds_read_b64_tr_b16 v[242:243], v202 offset:4608\n\t"
      "v_mfma_f32_16x16x32_bf16 %1, v[220:223], v[208:211], %1\n\t"
      "ds_read_b64_tr_b16 v[244:245], v203 offset:4096\n\t"
      "ds_read_b64_tr_b16 v[246:247], v203 offset:4608\n\t"
      "v_mfma_f32_16x16x32_bf16 %5, v[220:223], v[212:215], %5\n\t"
      "ds_read_b64_tr_b16 v[248:249], v204 offset:4096\n\t"
      "ds_read_b64_tr_b16 v[250:251], v204 offset:4608\n\t"
      "s_waitcnt lgkmcnt(8)\n\t"
      "v_mfma_f32_16x16x32_bf16 %2, v[224:227], v[208:211], %2\n\t"
      "ds_read_b64_tr_b16 v[252:253], v205 offset:4096\n\t"
      "v_mfma_f32_16x16x32_bf16 %6, v[224:227], v[212:215], %6\n\t"
      "ds_read_b64_tr_b16 v[254:255], v205 offset:4608\n\t"
      "v_mfma_f32_16x16x32_bf16 %3, v[228:231], v[208:211], %3\n\t"
      "v_mfma_f32_16x16x32_bf16 %7, v[228:231], v[212:215], %7\n\t"
      "s_barrier\n\t"
      "s_waitcnt lgkmcnt(4)\n\t"
      "v_mfma_f32_16x16x32_bf16 %0, v[240:243], v[232:235], %0\n\t"
      "v_mfma_f32_16x16x32_bf16 %4, v[240:243], v[236:239], %4\n\t"
      "v_mfma_f32_16x16x32_bf16 %1, v[244:247], v[232:235], %1\n\t"
      "v_mfma_f32_16x16x32_bf16 %5, v[244:247], v[236:239], %5\n\t"
      "s_waitcnt lgkmcnt(0)\n\t"
      "v_mfma_f32_16x16x32_bf16 %2, v[248:251], v[232:235], %2\n\t"
      "v_mfma_f32_16x16x32_bf16 %6, v[248:251], v[236:239], %6\n\t"
      "v_mfma_f32_16x16x32_bf16 %3, v[252:255], v[232:235], %3\n\t"
      "v_mfma_f32_16x16x32_bf16 %7, v[252:255], v[236:239], %7\n\t"
      "s_nop 7\n\t"
      "s_nop 1\n\t"
      : "+v"(acc[0][0]), "+v"(acc[0][1]), "+v"(acc[0][2]), "+v"(acc[0][3]), "+v"(acc[1][0]), "+v"(acc[1][1]), "+v"(acc[1][2]), "+v"(acc[1][3])
      : "v"(a0), "v"(trbase), "v"(f5)
      : "memory", "v198", "v199", "v200", "v201", "v202", "v203", "v204", "v205", "v206", "v207", "v208", "v209", "v210", "v211", "v212", "v213", "v214", "v215", "v216", "v217", "v218", "v219", "v220", "v221", "v222", "v223", "v224", "v225", "v226", "v227", "v228", "v229", "v230", "v231", "v232", "v233", "v234", "v235", "v236", "v237", "v238", "v239", "v240", "v241", "v242", "v243", "v244", "v245", "v246", "v247", "v248", "v249", "v250", "v251", "v252", "v253", "v254", "v255");
}


__global__ __launch_bounds__(512, 2) void fused(const char* __restrict__ x, const char* __restrict__ win, const char* __restrict__ wdr,
                                                const char* __restrict__ wdi, const char* __restrict__ gw, const char* __restrict__ wsp,
                                                const char* __restrict__ wsr, const char* __restrict__ wsi, const u16* __restrict__ bsp,
                                                const char* __restrict__ wout, char* ws, char* __restrict__ out) {
  __shared__ __attribute__((aligned(1024))) char smem[LDS_BYTES];
  const u32 lb = (u32)(size_t)(LAS char*)smem;
  u64 pc0; u32 rest;
  asm volatile("s_getpc_b64 %0\n.Lcp_anchor:\n\ts_mov_b32 %1, .Lcp_end-.Lcp_anchor" : "=s"(pc0), "=s"(rest));
  const int xcd = blockIdx.x & 7, idx = blockIdx.x >> 3;
  const int tid = threadIdx.x, wid = __builtin_amdgcn_readfirstlane(tid >> 6), lane = tid & 63;
  const int lw = wid & 3;
  const bool p1 = idx < 28;
  const int r1 = 2 * xcd + (idx >= 14 ? 1 : 0), c1 = idx >= 14 ? idx - 14 : idx;
  const int k0 = xcd == 7 ? 0 : xcd;
  auto kof = [&](int s) { int k = k0 + s; return k >= 7 ? k - 7 : k; };
  constexpr int WB = 114688;
  if (wid == 6) {
    const u32 o = (u32)(idx + 32 * lane) * 128;
    touch((const char*)pc0, o + 4 <= rest ? o : 0, lb + L_DUMMY + 512);
  }
  const int l3 = lane >> 3;
  const u32 rowl = 32 * lw + 8 * (l3 >> 2) + (l3 & 3);
  const u32 sw0 = ((lane & 7) ^ (l3 >> 1)) << 4;
  const u32 ldsA = lb + lw * 4096, ldsW = lb + WB + lw * 2048;
  u32 xo[4], wo[2];
  const char* const wz2 = win + 1792 + 128 * c1;
  if (wid < 4 && p1) {
    xo[0] = (128 * r1 + rowl) * 896 + sw0; xo[1] = (xo[0] ^ 64) + (16 * 896 - 1024); xo[2] = xo[0] + (4 * 896 - 2048); xo[3] = (xo[0] ^ 64) + (20 * 896 - 3072);
    wo[0] = (u32)((16 * lw + l3) * 3584) + (((lane & 7) ^ (l3 & 2)) << 4); wo[1] = (wo[0] ^ 64) + (8 * 3584 - 1024);
    if (wid == 3) flags_to_lds(ws + OFF_EPOCH, 0, lb + L_DUMMY + 256);
#pragma unroll
    for (int u = 0; u < 2; ++u) {
      const int k = kof(u);
      glds4(x + k * 128, xo[0], xo[1], xo[2], xo[3], ldsA + u * 16384);
      glds2(wz2 + (size_t)k * (64 * 3584), wo[0], wo[1], ldsW + u * 8192);
    }
  }
  SCHED();
  const bool isB = (xcd & 1) == 0;
  const int r = isB ? 4 * (xcd >> 1) + (idx >> 3) : 8 * ((xcd >> 1) & 1) + (idx >> 2);
  const int c = isB ? (idx & 7) : 8 + (xcd & 4) + (idx & 3);
  const int q = lane >> 4, i16 = lane & 15;
  const rsrc_t rs = __builtin_amdgcn_make_buffer_rsrc(ws, 0, WS_BYTES, 0x00020000);
  const int rb = xcd * 4 + idx / 7, db = idx % 7;
  u32 tag;
  const int cls = (r + c) & 3, lim = 28 + cls;
  auto perm = [&](int p) { int n = p - 1; n += (n >= lim) ? 1 : 0; const int v = n + (((n & 3) == cls) ? 4 : 0);
    return p < 2 ? p : p == 2 ? (cls < 2 ? cls + 4 : cls) : v; };

  if (wid < 4) {
    const char* const W = isB ? wsp : (c < 12) ? wsr : wsi;
    const int bofs = lw < 2 ? lw * 4096 : 2048 + lw * 3072;
    const u32 ldsB35 = lb + 16384 + bofs;
    if (p1) {
      u32 dof[2];
      auto issue = [&](int u) {
        const int k = kof(u % 7);
        if (u < 7) glds4(x + k * 128, xo[0], xo[1], xo[2], xo[3], ldsA + u * 16384);
        if (u < 7 || u >= 14) glds2(wz2 - (u >= 14 ? 1792 : 0) + (size_t)k * (64 * 3584), wo[0], wo[1], ldsW + (u % 5) * 8192);
        else glds2((c1 < 7 ? wdr + c1 * (64 * 896) : wdi + (c1 - 7) * (64 * 896)) + k * 128, dof[0], dof[1], ldsW + (u % 5) * 8192);
      };
      dof[0] = (u32)((16 * lw + l3) * 896) + sw0; dof[1] = (dof[0] ^ 64) + (8 * 896 - 1024);
      WAIT_V(6); BAR(); issue(2); issue(3);
#define NU(v) ((v) < 7 ? 6 : (v) < 21 ? 2 : 0)
#pragma unroll
      for (int u = 0; u < 7; ++u) { WAIT_V(NU(u + 2) + NU(u + 3)); BAR(); issue(u + 4); }
      tag = __builtin_amdgcn_readfirstlane(*(volatile LAS u32*)(LAS char*)(smem + L_DUMMY + 256)) + 1u;
      PIN_S(tag);
      if (wid == 0) *(volatile LAS u32*)(LAS char*)(smem + L_GO) = ~tag;
      {
        const u32 xch = (u32)OFF_XCH + r1 * (14 * 2048);
        const int row = 32 * lw + (lane & 31), g = lane >> 5;
        u32x4 gr[7];
#pragma unroll
        for (int u = 7; u < 14; ++u) {
          if (u < 13) WAIT_V(u > 9 ? 11 : 4);
          BAR();
          if (u < 12) issue(u + 4);
          if (u == 9) {
#pragma unroll
            for (int k = 0; k < 7; ++k) gr[k] = ld16(rs, xch + (7 * g + k) * 2048 + row * 16);
            SCHED();
          }
        }
        WAIT_V(0);
#pragma unroll
        for (int k = 0; k < 7; ++k) asm volatile("" : "+v"(gr[k]));
        auto bad = [&]() { u32 b_ = 0;
#pragma unroll
          for (int k = 0; k < 7; ++k) b_ |= (gr[k].y ^ tag) | (gr[k].w ^ tag);
          return __any(b_ != 0); };
        if (bad()) {
          const u64 t0 = __builtin_amdgcn_s_memrealtime();
          do {
            asm volatile("" ::: "memory");
#pragma unroll
            for (int k = 0; k < 7; ++k) gr[k] = ld16(rs, xch + (7 * g + k) * 2048 + row * 16);
          } while (bad() && __builtin_amdgcn_s_memrealtime() - t0 < 2000000ull);
        }
        float ms = 0.f;
#pragma unroll
        for (int k = 0; k < 7; ++k) ms += __uint_as_float(gr[k].x);
        const float mean = half_sum(ms) * (1.f / 14.f);
        float m2 = 0.f;
#pragma unroll
        for (int k = 0; k < 7; ++k) { const float d = __uint_as_float(gr[k].x) - mean; m2 += __uint_as_float(gr[k].z) + 64.f * d * d; }
        m2 = half_sum(m2);
        const float rstd = __builtin_amdgcn_rsqf(m2 * (1.f / 896.f) + 1e-5f);
        ((LAS float*)(LAS char*)(smem + L_FIN))[g * 128 + row] = g ? -mean * rstd : rstd;
      }
      WAIT_L0();
      BAR();
      issue(16); issue(17);
#pragma unroll
      for (int u = 14; u < 21; ++u) {
        WAIT_V(NU(u + 2) + NU(u + 3)); BAR();
        if (u == 14 + VMS && wid == 0) put_flag(rs, (u32)OFF_F1 + (c1 * 16 + r1) * FS, tag, lane);
        if (u + 4 < 21) issue(u + 4);
      }
    } else {
      tag = __builtin_amdgcn_readfirstlane(ld4(rs, (u32)OFF_EPOCH)) + 1u;
      const int wv = 4 * (idx - 28) + lw;
      const char* Wp = isB ? wsp + (size_t)(512 * (xcd >> 1) + 32 * wv) * 4096
                           : ((xcd & 4) ? wsi : wsr) + (size_t)(1024 * ((xcd >> 1) & 1) + 64 * wv) * 4096;
      const u32 pv = (u32)((lane >> 2) * 4096 + (lane & 3) * 128);
      const u64 t0 = __builtin_amdgcn_s_memrealtime();
      while (__builtin_amdgcn_s_memrealtime() - t0 < 50) __builtin_amdgcn_s_sleep(4);
      {
        const u32 gl = (u32)((16 * xcd + wv) * 64 + lane), dm = lb + L_DUMMY + lw * 256;
        const int gw_ = 16 * xcd + wv;
        if (gw_ < 49) touch(wdr, gl * 128, dm); else if (gw_ < 98) touch(wdi, (gl - 3136) * 128, dm);
        if (gw_ < 98) { const u32 rw = gl / 14; touch(win, rw * 3584 + (gl - rw * 14) * 128, dm); touch(wout, gl * 128, dm); }
      }
      const char* Wq = ((xcd & 4) ? wsi : wsr) + (size_t)(1024 * ((xcd >> 1) & 1) + 64 * wv) * 4096 + 3 * 512;
#pragma unroll 1
      for (int l = 0; l < 24; ++l) {
        const int l2 = l - 8;
        const char* pp_ = !isB ? Wp + (l & 3) * 65536 + (l >> 2) * 512 : l2 < 0 ? Wp + (l & 1) * 65536 + (l >> 1) * 512 : Wq + (l2 & 3) * 65536 + (l2 >> 2) * 512;
        touch(pp_, pv, lb + L_DUMMY + lw * 256);
        WAIT_V(1);
        if (__builtin_amdgcn_s_memrealtime() - t0 > 650) break;
      }
    }
    u32 ao[4];
    ao[0] = (128 * r + rowl) * 4096 + sw0; ao[1] = (ao[0] ^ 64) + (16 * 4096 - 1024); ao[2] = ao[0] + (4 * 4096 - 2048); ao[3] = (ao[0] ^ 64) + (20 * 4096 - 3072);
    const char* Bm = ws + OFF_BMAT + (size_t)c * BMAT_C + bofs;
    const u32 boff = lane * 16;
    auto issueA = [&](int t, int slot) { glds4(W + t * 128, ao[0], ao[1], ao[2], ao[3], ldsA + slot * SLOT); };
    issueA(0, 0); issueA(1, 1);
    {
      const int glo = (7 * (c & 7)) >> 2, ghi = (7 * (c & 7) + 6) >> 2;
      volatile LAS u32* go = (volatile LAS u32*)(LAS char*)(smem + L_GO);
      if (!p1 || wid == 0) {
        wait_flags(rs, (u32)OFF_F1 + glo * 16 * FS, (ghi - glo + 1) * 16, tag, lane);
        if (p1) *go = tag;
      } else {
        const u64 t0 = __builtin_amdgcn_s_memrealtime();
        while ((u32)__builtin_amdgcn_readfirstlane(*go) != tag && __builtin_amdgcn_s_memrealtime() - t0 < 2100000ull) __builtin_amdgcn_s_sleep(1);
      }
    }
    auto stream = [&](auto nbc) {
    constexpr int NB = decltype(nbc)::value, N = 4 + NB;
    auto issueB = [&](int t, int slot) { gldsB<NB>(Bm + (size_t)t * 14336, boff, ldsB35 + slot * SLOT); };
    auto issueG = [&](int p, int slot) { const int t = perm(p); issueA(t, slot); issueB(t, slot); };
    issueB(0, 0); issueB(1, 1); issueG(2, 2);
    WAIT_V(NB + N); BAR();
    issueG(3, 3);
    WAIT_V(2 * N); BAR(); issueG(4, 4);
    WAIT_V(2 * N); BAR(); issueG(5, 0); issueG(6, 1);
    u32 so = 2 * SLOT;
    auto steps = [&](int p0, int p1_) {
#pragma unroll 1
      for (int p = p0; p < p1_; ++p) {
        const int t = perm(p + 5);
        const char* pa = W + t * 128; const char* pb = Bm + (size_t)t * 14336; u32 da = ldsA + so, dbb = ldsB35 + so;
        PIN_S(pa); PIN_S(pb); PIN_S(da); PIN_S(dbb);
        WAIT_V(3 * N);
        BAR();
        glds4(pa, ao[0], ao[1], ao[2], ao[3], da); gldsB<NB>(pb, boff, dbb);
        so = (so == 4 * SLOT) ? 0 : so + SLOT;
      }
    };
    steps(2, 4);
    if (wid == 0) put_flag(rs, p1 ? (u32)OFF_F1Z + (r1 * 16 + c1) * FS : (u32)OFF_FG + (4 * xcd + idx - 28) * FS, tag, lane);
    steps(4, 13);
    if (isB && wid == 0) flags_to_lds(ws + OFF_F1Z + r * 16 * FS, (lane < 14 ? lane : 0) * FS, lb + L_DUMMY);
    steps(13, 18);
    if (isB && wid == 0) { const u32 fz = ((const volatile LAS u32*)(LAS char*)(smem + L_DUMMY))[lane]; if (!__all(fz == tag)) wait_flags(rs, (u32)OFF_F1Z + r * 16 * FS, 14, tag, lane); }
    steps(18, 27);
    if (p1) {
      const u32 wq = (u32)((56 * lw + l3) * 896 + db * 128 + (((lane & 7) ^ (((l3 >> 1) & 3) << 1)) << 4));
      auto issueW = [&](int i, int slot_) {
#pragma unroll
        for (int k = 0; k < 7; ++k) glds(wout + (size_t)(224 * i + 8 * k) * 896, wq, lb + slot_ * SLOT + (7 * lw + k) * 1024);
      };
      WAIT_V(3 * N); BAR(); issueW(0, 2);
      WAIT_V(2 * N + 7); BAR(); issueW(1, 3);
      WAIT_V(N + 14); BAR(); issueW(2, 4);
      WAIT_V(21); BAR(); issueW(3, 0);
    } else {
      WAIT_V(3 * N); BAR();
      WAIT_V(2 * N); BAR();
      WAIT_V(N);  BAR();
      WAIT_V(0);  BAR();
    }
    };
    if (lw < 2) stream(IC<4>{}); else stream(IC<3>{});
    BAR();
  } else {
    if (wid == 5 && idx == 28) {
      const char* Wt = (isB ? wsp + (size_t)(512 * (xcd >> 1)) * 4096 : ((xcd & 4) ? wsi : wsr) + (size_t)(1024 * ((xcd >> 1) & 1)) * 4096);
      const char* p = lane < 20 ? ws + min((u32)lane << 20, WS_BYTES - 4) : lane < 23 ? out + min((lane - 20) << 20, 2048 * 896 - 4)
                    : lane < 25 ? wout + (lane - 23) * (896 * 896 - 4) : lane < 27 ? wdr + (lane - 25) * (448 * 896 - 4) : lane < 29 ? wdi + (lane - 27) * (448 * 896 - 4)
                    : lane < 32 ? win + min((lane - 29) << 20, 448 * 3584 - 4) : Wt + min((lane - 32) << 20, (isB ? 2 << 20 : 4 << 20) - 4);
      touch_ptr(p, lb + L_DUMMY + 768);
    }
    u32 bs[2];
#pragma unroll
    for (int m = 0; m < 2; ++m) bs[m] = (u32)bsp[128 * r + 32 * lw + 8 * (i16 >> 2) + 4 * m + (i16 & 3)];
    if (p1) {
      f32x4 az[2][4], ab[2][4];
#define ZERO8(A) do { _Pragma("unroll") for (int m = 0; m < 2; ++m) _Pragma("unroll") for (int j = 0; j < 4; ++j) A[m][j] = f32x4{0.f, 0.f, 0.f, 0.f}; } while (0)
#define TRB1() (lb + WB + (8 * q + (i16 >> 2)) * 128 + (lane & 3) * 8)
#define F51() ((((i16 >> 3) & 1) | ((q & 1) << 1)) << 5)
      auto tileoff = [&](int j) { const int nt = 4 * c1 + j, g7 = (nt * 37) >> 8; return (u32)(g7 * (int)BMAT_C + (nt - 7 * g7) * 1024); };
      ZERO8(az);
      BAR();
      pass_z2(az, A0(), TRB1(), F51());
      tag = __builtin_amdgcn_readfirstlane(*(volatile LAS u32*)(LAS char*)(smem + L_DUMMY + 256)) + 1u;
      {
        LANES();
#pragma unroll
        for (int j = 0; j < 4; ++j) { az[0][j] = gelu4(az[0][j]); az[1][j] = gelu4(az[1][j]); }
        f32x4 s0 = (az[0][0] + az[0][1]) + (az[0][2] + az[0][3]), s1 = (az[1][0] + az[1][1]) + (az[1][2] + az[1][3]);
        row16_sum8(s0, s1);
        s0 *= (1.f / 64.f); s1 *= (1.f / 64.f);
        f32x4 v0 = {0.f, 0.f, 0.f, 0.f}, v1 = {0.f, 0.f, 0.f, 0.f};
#pragma unroll
        for (int j = 0; j < 4; ++j) { const f32x4 e0 = az[0][j] - s0, e1 = az[1][j] - s1; v0 += e0 * e0; v1 += e1 * e1; }
        row16_sum8(v0, v1);
        const f32x4 sm = (i16 & 4) ? s1 : s0, vm = (i16 & 4) ? v1 : v0;
        const float sa = (i16 & 2) ? ((i16 & 1) ? sm[3] : sm[2]) : ((i16 & 1) ? sm[1] : sm[0]);
        const float va = (i16 & 2) ? ((i16 & 1) ? vm[3] : vm[2]) : ((i16 & 1) ? vm[1] : vm[0]);
        if (i16 < 8) st16(rs, (u32)OFF_XCH + r1 * (14 * 2048) + c1 * 2048 + (32 * lw + 8 * q + i16) * 16, u32x4{__float_as_uint(sa), tag, __float_as_uint(va), tag});
        ZERO8(ab);
        pass_ab(ab, A0(), lb + WB + i16 * 128 + ((q ^ ((i16 >> 1) & 7)) << 4));
      }
      {
        LANES();
        const u32 bo = (u32)OFF_BMAT + ((4 * r1 + lw) * 7) * 1024 + lane * 16;
#pragma unroll
        for (int j = 0; j < 4; ++j)
          st16(rs, bo + 8 * (u32)BMAT_C + tileoff(j), u32x4{pk2(ab[0][j][0], ab[0][j][1]), pk2(ab[0][j][2], ab[0][j][3]), pk2(ab[1][j][0], ab[1][j][1]), pk2(ab[1][j][2], ab[1][j][3])});
        SCHED();
        BAR();
        const LAS f32x4* fin = (const LAS f32x4*)(const LAS char*)(smem + L_FIN);
        const f32x4 r0 = fin[8 * lw + 2 * q], r1_ = fin[8 * lw + 2 * q + 1], h0 = fin[32 + 8 * lw + 2 * q], h1 = fin[32 + 8 * lw + 2 * q + 1];
#pragma unroll
        for (int j = 0; j < 4; ++j) {
          const f32x4 u0 = az[0][j] * r0 + h0, u1 = az[1][j] * r1_ + h1;
          st16(rs, bo + tileoff(j), u32x4{pk2(u0[0], u0[1]), pk2(u0[2], u0[3]), pk2(u1[0], u1[1]), pk2(u1[2], u1[3])});
        }
        ZERO8(ab);
        pass_z1(ab, A0(), TRB1(), F51());
      }
      {
        LANES();
#pragma unroll
        for (int m = 0; m < 2; ++m)
#pragma unroll
          for (int k = 0; k < 2; ++k) {
            const f32x4 g0 = gelu4(ab[m][2 * k]), g1 = gelu4(ab[m][2 * k + 1]);
            st16(rs, (u32)OFF_Z1 + (8 * r1 + 2 * lw + m) * (28 * 1024) + (2 * c1 + k) * 1024 + lane * 16, u32x4{pk2(g0[0], g0[1]), pk2(g0[2], g0[3]), pk2(g1[0], g1[1]), pk2(g1[2], g1[3])});
          }
      }
    } else {
      tag = __builtin_amdgcn_readfirstlane(ld4(rs, (u32)OFF_EPOCH)) + 1u;
      LANES();
      const int row0 = 64 * (4 * xcd + idx - 28) + 16 * lw;
      const int lc = lane < 56 ? lane : 0;
      const u32x4 gv = *(const u32x4*)(gw + lc * 16);
      float mine = 0.f;
      u32x4 xa[16];
#pragma unroll
      for (int k = 0; k < 16; ++k) xa[k] = *(const u32x4*)(x + (size_t)(row0 + k) * 896 + lc * 16);
      SCHED();
#pragma unroll
      for (int k = 0; k < 16; ++k) {
        const u32x4 xv = xa[k];
        float d = 0.f;
#pragma unroll
        for (int e = 0; e < 4; ++e) d += blo(xv[e]) * blo(gv[e]) + bhi(xv[e]) * bhi(gv[e]);
        d = lane < 56 ? d : 0.f;
        d = dpp_add<0x121>(dpp_add<0x122>(dpp_add<0x124>(dpp_add<0x128>(d))));
        const auto sw_ = __builtin_amdgcn_permlane16_swap(__float_as_uint(d), __float_as_uint(d), false, false);
        const float t = half_sum(__uint_as_float(sw_[0]) + __uint_as_float(sw_[1]));
        mine = lane == k ? t : mine;
      }
      if (lane < 16) __builtin_amdgcn_raw_buffer_store_b32(__float_as_uint(__builtin_amdgcn_rcpf(1.0f + __builtin_amdgcn_exp2f(-1.442695041f * mine))), rs, (u32)OFF_GATE + (row0 + lane) * 4, 0, 16);
    }
    f32x4 acc[2][7];
#pragma unroll
    for (int m = 0; m < 2; ++m) {
      const float bb = isB ? __uint_as_float(bs[m] << 16) : 0.f;
#pragma unroll
      for (int j = 0; j < 7; ++j) acc[m][j] = f32x4{bb, bb, bb, bb};
    }
    SCHED();
    BAR();
    const int c7 = 7 * c;
#define EPI(PAR, TILE)                                                                                                     \
      _Pragma("unroll") for (int m = 0; m < 2; ++m) {                                                                      \
        _Pragma("unroll") for (int k = 0; k < 3; ++k) {                                                                    \
          u32x2 lo_, hi_;                                                                                                  \
          { const int j = 2 * k + PAR; lo_ = TILE; } { const int j = 2 * k + 1 + PAR; hi_ = TILE; }                        \
          st16(rs, so_ + m * (56 * 1024) + (k + PAR) * 1024, u32x4{lo_.x, lo_.y, hi_.x, hi_.y});                           \
          SCHED();                                                                                                         \
        }                                                                                                                  \
        { const int j = PAR ? 0 : 6; st8(rs, so_ + m * (56 * 1024) + (PAR ? 512 : 3072) - lane * 8, TILE); }               \
      }
    if (isB) {
      u32x4 zz[2][4];
      { LANES(); loop_gz(acc, zz, 32, A0(), lb + 16384 + lane * 16, (u32)((8 * r + 2 * lw) * (28 * 1024) + lane * 16 + (c7 >> 1) * 1024), ws + OFF_Z1); }
      LANES();
      const u32 so_ = (u32)OFF_OMAT + (8 * r + 2 * lw) * (56 * 1024) + (c7 >> 1) * 1024 + lane * 16;
      auto tile = [&](int m, int j, int zi) {
        const u32 z0 = (zi & 1) ? zz[m][zi >> 1].z : zz[m][zi >> 1].x, z1 = (zi & 1) ? zz[m][zi >> 1].w : zz[m][zi >> 1].y;
        const f32x4 hv = acc[m][j] * f32x4{blo(z0), bhi(z0), blo(z1), bhi(z1)};
        return u32x2{pk2(hv[0], hv[1]), pk2(hv[2], hv[3])};
      };
      if ((c & 1) == 0) { EPI(0, tile(m, j, j)) } else { EPI(1, tile(m, j, j + 1)) }
    } else {
      { LANES(); loop_g(acc, 32, A0(), lb + 16384 + lane * 16); }
      LANES();
      const u32 so_ = (u32)OFF_OMAT + (8 * r + 2 * lw) * (56 * 1024) + (c7 >> 1) * 1024 + lane * 16;
      auto tile = [&](int m, int j) { return u32x2{pk2(acc[m][j][0], acc[m][j][1]), pk2(acc[m][j][2], acc[m][j][3])}; };
      if ((c & 1) == 0) { EPI(0, tile(m, j)) } else { EPI(1, tile(m, j)) }
    }
    WAIT_V(0);
  }
  WAIT_L0();
  BAR();
  if (wid == 4) put_flag(rs, (u32)OFF_F2 + (r * 16 + c) * FS, tag, fresh_lane());
  if (!p1) {
    if (blockIdx.x == 255 && wid == 0) {
      LANES();
#pragma unroll 1
      for (int k = 0; k < 4; ++k) wait_flags(rs, (u32)OFF_F2 + k * 64 * FS, 64, tag, lane);
      if (lane == 0) __hip_atomic_store((GAS u32*)(ws + OFF_EPOCH), tag, RLX_AGENT);
    }
    return;
  }

  {
    if (wid < 4) WAIT_V(0);
    BAR();
    LANES();
    constexpr int s0 = 2;
    const int tq = wid & 3, kh = wid >> 2;
    const int T = 4 * rb + tq;
    const int a = i16 >> 2, pp = lane & 3;
    const int rloc = 4 * q + a;
    const int sw = ((rloc >> 1) & 3) << 1;
    const char* tb0 = smem + ((s0 + 2 * kh) % 5) * SLOT + rloc * 128 + (pp & 1) * 8;
    const char* tb1 = smem + ((s0 + 2 * kh + 1) % 5) * SLOT + rloc * 128 + (pp & 1) * 8;
    int co[4];
#pragma unroll
    for (int j = 0; j < 4; ++j) co[j] = ((2 * j + (pp >> 1)) ^ sw) << 4;
    bf16x8 wf[7][4];
#pragma unroll
    for (int p = 0; p < 7; ++p)
#pragma unroll
      for (int j = 0; j < 4; ++j) wf[p][j] = lds_tr2(tb0 + p * 4096 + co[j], tb0 + p * 4096 + co[j] + 2048);
    const u32 om = (u32)OFF_OMAT + T * (56 * 1024) + lane * 16;
    u32 hb[4], hs[2];
#pragma unroll
    for (int i = 0; i < 4; ++i) hb[i] = om + (14 * kh + 4 * i) * 1024;
    hs[0] = om + (14 * kh + 3) * 1024 - lane * 8; hs[1] = hs[0] + 7 * 1024;
    const int yp = 2 * db + kh; const bool ysp = yp == 3 || yp == 10;
    u32 yo0 = om + (28 + yp) * 1024 - (ysp ? lane * 8 : 0), yo1 = yo0 + 14 * 1024;
    const int trow = 32 * (T >> 1) + 8 * (i16 >> 2) + 4 * (T & 1) + (i16 & 3);
    char* red = smem + ((s0 + 4) % 5) * SLOT;
    u32 wa = (u32)(kh * 16384 + (trow & 63) * 256 + ((q ^ (i16 & 3)) << 4)), wx = (i16 & 4) << 4;
    const int rr = 8 * wid + (lane >> 3);
    u32 ra = (u32)(rr * 256 + ((((lane & 7) * 2) ^ ((rr & 3) | ((rr >> 1) & 4))) << 4));
    u32 oo = (u32)((64 * rb + rr) * 896 + db * 128 + (lane & 7) * 16);
    asm volatile("" : "+v"(wa), "+v"(wx), "+v"(ra), "+v"(oo));
    u32 go_ = (u32)OFF_GATE + trow * 4;
    asm volatile("" : "+v"(hb[0]), "+v"(hb[1]), "+v"(hb[2]), "+v"(hb[3]), "+v"(hs[0]), "+v"(hs[1]), "+v"(yo0), "+v"(yo1), "+v"(go_));
    u32x4 hf[14];
    auto ldh = [&](int p) { return (p % 7 == 3) ? ld8x2(rs, hs[p / 7], hs[p / 7] + 512) : ld16(rs, hb[p >> 2] + (p & 3) * 1024); };
    SCHED();
    if (wid == 0) {
      const u32 fo = lane < 16 ? (u32)OFF_F2 + ((rb >> 1) * 16 + lane) * FS : (u32)OFF_FG + rb * FS;
      const u64 t0 = __builtin_amdgcn_s_memrealtime();
      for (;;) {
        asm volatile("" ::: "memory");
        if (__all(ld4(rs, fo) == tag)) break;
        if (__builtin_amdgcn_s_memrealtime() - t0 > 2000000ull) break;
        __builtin_amdgcn_s_sleep(1);
      }
    }
    BAR();
#pragma unroll
    for (int p = 0; p < HD; ++p) hf[p] = ldh(p);
    SCHED();
    u32x4 yr, yi; u32 gt; float og; f32x4 ya[4];
    f32x4 acc[4];
#pragma unroll
    for (int j = 0; j < 4; ++j) acc[j] = f32x4{0.f, 0.f, 0.f, 0.f};
    SCHED();
#pragma unroll
    for (int p = 0; p < 14; ++p) {
#pragma unroll
      for (int j = 0; j < 4; ++j) acc[j] = MFMA(wf[p % 7][j], __builtin_bit_cast(bf16x8, hf[p]), acc[j]);
      SCHED();
      if (p + HD < 14) hf[p + HD] = ldh(p + HD);
      if (p == 3) { if (ysp) yr = ld8x2(rs, yo0, yo0 + 512); else yr = ld16(rs, yo0); gt = ld4(rs, go_); }
      if (p == 4) { if (ysp) yi = ld8x2(rs, yo1, yo1 + 512); else yi = ld16(rs, yo1); }
      if (p < 7) {
#pragma unroll
        for (int j = 0; j < 4; ++j) wf[p][j] = lds_tr2(tb1 + p * 4096 + co[j], tb1 + p * 4096 + co[j] + 2048);
      }
      if (p == 11) {
        const float gate = __uint_as_float(gt);
        og = 1.0f - gate;
        const f32x4 zero4 = {0.f, 0.f, 0.f, 0.f};
        const f32x4 y0 = f32x4{blo(yr.x) - blo(yi.x), bhi(yr.x) - bhi(yi.x), blo(yr.y) - blo(yi.y), bhi(yr.y) - bhi(yi.y)} * gate;
        const f32x4 y1 = f32x4{blo(yr.z) - blo(yi.z), bhi(yr.z) - bhi(yi.z), blo(yr.w) - blo(yi.w), bhi(yr.w) - bhi(yi.w)} * gate;
#pragma unroll
        for (int j = 0; j < 4; ++j) { ya[j] = (j >> 1) == kh ? ((j & 1) ? y1 : y0) : zero4; asm volatile("" : "+v"(ya[j])); }
      }
      SCHED();
    }
#pragma unroll
    for (int j = 0; j < 4; ++j) *(LAS f32x4*)(LAS char*)(red + wa + ((j * 64) ^ wx)) = acc[j] * og + ya[j];
    WAIT_L0();
    BAR();
    {
      const f32x4 o0 = *(const LAS f32x4*)(const LAS char*)(red + ra) + *(const LAS f32x4*)(const LAS char*)(red + ra + 16384);
      const f32x4 o1 = *(const LAS f32x4*)(const LAS char*)(red + (ra ^ 16)) + *(const LAS f32x4*)(const LAS char*)(red + (ra ^ 16) + 16384);
      const rsrc_t ro = __builtin_amdgcn_make_buffer_rsrc(out, 0, 2048 * 896, 0x00020000);
      __builtin_amdgcn_raw_buffer_store_b128(u32x4{pk2(o0[0], o0[1]), pk2(o0[2], o0[3]), pk2(o1[0], o1[1]), pk2(o1[2], o1[3])}, ro, oo, 0, 16);
    }
    asm volatile(".Lcp_end:");
  }
}

extern "C" void kernel_launch(void* const* d_in, const int* in_sizes, int n_in, void* d_out, int out_size, void* d_ws, size_t ws_size,
                              hipStream_t stream) {
  fused<<<256, 512, 0, stream>>>((const char*)d_in[0], (const char*)d_in[6], (const char*)d_in[3], (const char*)d_in[4], (const char*)d_in[10],
                                 (const char*)d_in[7], (const char*)d_in[1], (const char*)d_in[2], (const u16*)d_in[8], (const char*)d_in[9],
                                 (char*)d_ws, (char*)d_out);
}
```
